```python
import torch

def _softmax(x, axis=-1):
    return torch.softmax(x, dim=axis)

def _gelu(x):
    return torch.nn.functional.gelu(x, approximate='tanh')

def _silu(x):
    return torch.nn.functional.silu(x)

def _sigmoid(x):
    return torch.sigmoid(x)

def _layernorm(x, axis=-1, eps=1e-05):
    m = x.mean(dim=axis, keepdim=True)
    v = x.var(dim=axis, keepdim=True, unbiased=False)
    return (x - m) / torch.sqrt(v + eps)

def _scatter_add(tgt, idx, src, axis=0):
    return tgt.clone().index_add_(axis, idx, src)

def _segment_sum(src, seg, n):
    out = torch.zeros((n,) + src.shape[1:], dtype=src.dtype)
    return out.index_add_(0, seg, src)

def _pad(x, pw):
    flat = tuple((v for pair in reversed(pw) for v in pair))
    return torch.nn.functional.pad(x, flat)
kernel_name = 'adaLN_cond__linear_kernel_hedgehog__glu_gated_reglu__prompt'

def setup_inputs():

    _w = lambda a: (lambda t: t.to(torch.bfloat16) if t.is_floating_point() else t)(torch.as_tensor(a))

    def _A_setup():
        B, S, D, H = (1, 2048, 512, 32)
        Dh = D // H
        return {'x': _w((lambda *s: torch.randn(*s, dtype=torch.bfloat16) * 0.05)(B, S, D)), 'wqkv': _w((lambda *s: torch.randn(*s, dtype=torch.bfloat16) * 0.4)(D, 3 * D)), 'w_phi': _w((lambda *s: torch.randn(*s, dtype=torch.bfloat16) * 0.4)(H, Dh, Dh) * Dh ** (-0.5)), 'wo': _w((lambda *s: torch.randn(*s, dtype=torch.bfloat16) * 0.05)(D, D))}
    _A_in = _A_setup()

    def _B_setup():
        B, S, D, F = (1, 2048, 512, 1376)
        return {'x': _w((lambda *s: torch.randn(*s, dtype=torch.bfloat16) * 0.05)(B, S, D)), 'W_gate': _w((lambda *s: torch.randn(*s, dtype=torch.bfloat16) * 0.8)(D, F) * (2.0 / D) ** 0.5), 'W_up': _w((lambda *s: torch.randn(*s, dtype=torch.bfloat16) * 0.8)(D, F) * (2.0 / D) ** 0.5), 'W_down': _w((lambda *s: torch.randn(*s, dtype=torch.bfloat16) * 0.8)(F, D) * (1.0 / F) ** 0.5)}
    _B_in = _B_setup()
    return {'A_x': _A_in['x'], 'A_wqkv': _A_in['wqkv'], 'A_w_phi': _A_in['w_phi'], 'A_wo': _A_in['wo'], 'B_x': _B_in['x'], 'B_W_gate': _B_in['W_gate'], 'B_W_up': _B_in['W_up'], 'B_W_down': _B_in['W_down'], 'c': _w((lambda *s: torch.randn(*s, dtype=torch.bfloat16) * 0.2)(1, 512) * 8.0), 'w_mod': _w((lambda *s: torch.randn(*s, dtype=torch.bfloat16) * 0.2)(512, 6 * 512) * 512 ** (-0.5) * 8.0)}

def reference(A_x, A_wqkv, A_w_phi, A_wo, B_x, B_W_gate, B_W_up, B_W_down, c, w_mod):

    def _A_ref(x, wqkv, w_phi, wo):
        B, S, D = x.shape
        H = 32
        Dh = D // H
        qkv = (lambda x, *d: x.permute(*d))((x @ wqkv).reshape(B, S, 3, H, Dh), 2, 0, 3, 1, 4)
        q, k, v = (qkv[0], qkv[1], qkv[2])
        pq = torch.einsum('bhsd,hdf->bhsf', q, w_phi)
        pk = torch.einsum('bhsd,hdf->bhsf', k, w_phi)
        phi_q = torch.concatenate([_softmax(pq, axis=-1), _softmax(-pq, axis=-1)], axis=-1)
        phi_k = torch.concatenate([_softmax(pk, axis=-1), _softmax(-pk, axis=-1)], axis=-1)
        kv = torch.einsum('bhsf,bhsd->bhfd', phi_k, v)
        z = torch.einsum('bhsf->bhf', phi_k)
        num = torch.einsum('bhqf,bhfd->bhqd', phi_q, kv)
        den = torch.einsum('bhqf,bhf->bhq', phi_q, z)[..., None] + 1e-06
        o = num / den
        return (lambda x, *d: x.permute(*d))(o, 0, 2, 1, 3).reshape(B, S, D) @ wo

    def _B_ref(x, W_gate, W_up, W_down):
        gate = torch.einsum('bsd,df->bsf', x, W_gate)
        up = torch.einsum('bsd,df->bsf', x, W_up)
        gate = gate * (lambda x, dt: x.to(dt))(gate > 0, gate.dtype)
        h = gate * up
        return torch.einsum('bsf,fd->bsd', h, W_down)
    x = A_x
    mod = (_silu(c) @ w_mod).reshape(c.shape[0], 6, 1, -1)
    sa, ca, ga, sb, cb, gb = (mod[:, 0].clone(), mod[:, 1].clone(), mod[:, 2].clone(), mod[:, 3].clone(), mod[:, 4].clone(), mod[:, 5].clone())
    h = _layernorm(x, axis=-1) * (1 + ca) + sa
    x = x + ga * _A_ref(h, A_wqkv, A_w_phi, A_wo)
    h = _layernorm(x, axis=-1) * (1 + cb) + sb
    x = x + gb * _B_ref(h, B_W_gate, B_W_up, B_W_down)
    return x
```

```cpp
#include <hip/hip_runtime.h>
#include <hip/hip_bf16.h>
typedef unsigned short u16;
__device__ __forceinline__ float bf2f(u16 b){ return __uint_as_float(((unsigned)b)<<16); }
__device__ __forceinline__ u16 f2bf(float f){ unsigned u=__float_as_uint(f); u += 0x7fff + ((u>>16)&1); return (u16)(u>>16); }
constexpr int S=2048, D=512, H=32, DH=16, F=1376;

__global__ void k_mod(const u16* c, const u16* wmod, float* mod){
  int j = blockIdx.x*blockDim.x+threadIdx.x; if(j>=6*D) return;
  float acc=0;
  for(int k=0;k<D;k++){ float cv=bf2f(c[k]); float s=cv/(1.f+__expf(-cv)); acc+=s*bf2f(wmod[(size_t)k*6*D+j]); }
  mod[j]=acc;
}
__global__ void k_ln(const u16* xb, const float* xf, const float* shift, const float* scale, u16* out){
  int r=blockIdx.x; int t=threadIdx.x; __shared__ float red[512];
  float v = xb? bf2f(xb[(size_t)r*D+t]) : xf[(size_t)r*D+t];
  red[t]=v; __syncthreads();
  for(int s=256;s>0;s>>=1){ if(t<s) red[t]+=red[t+s]; __syncthreads(); }
  float m=red[0]/D; __syncthreads();
  float d=v-m; red[t]=d*d; __syncthreads();
  for(int s=256;s>0;s>>=1){ if(t<s) red[t]+=red[t+s]; __syncthreads(); }
  float var=red[0]/D;
  float y=d*rsqrtf(var+1e-5f);
  out[(size_t)r*D+t]=f2bf(y*(1.f+scale[t])+shift[t]);
}
__global__ void k_gemm(const u16* A, const u16* B, float* C, int M,int N,int K){
  __shared__ float As[16][17], Bs[16][17];
  int tx=threadIdx.x&15, ty=threadIdx.x>>4; int row=blockIdx.y*16+ty, col=blockIdx.x*16+tx; float acc=0;
  for(int k0=0;k0<K;k0+=16){ As[ty][tx]=bf2f(A[(size_t)row*K+k0+tx]); Bs[ty][tx]=bf2f(B[(size_t)(k0+ty)*N+col]); __syncthreads();
    for(int k=0;k<16;k++) acc+=As[ty][k]*Bs[k][tx]; __syncthreads(); }
  C[(size_t)row*N+col]=acc;
}
__global__ void k_phi(const float* qkv, const u16* wphi, float* phi){
  int i=blockIdx.x*blockDim.x+threadIdx.x; int which=i/(S*H); int r=i%(S*H); int s=r/H, h=r%H;
  const float* q=qkv+(size_t)s*3*D+which*D+h*DH;
  float p[16]; float mx=-1e30f, mn=1e30f;
  #pragma unroll
  for(int f=0;f<16;f++){ float a=0;
    #pragma unroll
    for(int d=0;d<16;d++) a+=q[d]*bf2f(wphi[h*256+d*16+f]); p[f]=a; mx=fmaxf(mx,a); mn=fminf(mn,a);}
  float s1=0,s2=0;
  #pragma unroll
  for(int f=0;f<16;f++){ s1+=__expf(p[f]-mx); s2+=__expf(mn-p[f]); }
  float* o=phi+(((size_t)which*S+s)*H+h)*32;
  #pragma unroll
  for(int f=0;f<16;f++){ o[f]=__expf(p[f]-mx)/s1; o[16+f]=__expf(mn-p[f])/s2; }
}
__global__ void k_kv(const float* qkv, const float* phi, float* kv){
  int h=blockIdx.x; int t=threadIdx.x; if(t>=32*17) return; int f=t/17, d=t%17; float acc=0;
  for(int s=0;s<S;s++){ float pk=phi[(((size_t)S+s)*H+h)*32+f]; float v= d<16? qkv[(size_t)s*3*D+2*D+h*DH+d]:1.f; acc+=pk*v; }
  kv[h*32*17+t]=acc;
}
__global__ void k_o(const float* phi, const float* kv, u16* o){
  int i=blockIdx.x*blockDim.x+threadIdx.x; int s=i/D, c=i%D, h=c/16, d=c%16; float num=0, den=0;
  for(int f=0;f<32;f++){ float p=phi[((size_t)s*H+h)*32+f]; num+=p*kv[h*544+f*17+d]; den+=p*kv[h*544+f*17+16]; }
  o[i]=f2bf(num/(den+1e-6f));
}
__global__ void k_res1(const u16* x, const float* a, const float* g, float* x1){ int i=blockIdx.x*blockDim.x+threadIdx.x; x1[i]=bf2f(x[i])+g[i%D]*a[i]; }
__global__ void k_glu(const float* g, const float* u, u16* h){ int i=blockIdx.x*blockDim.x+threadIdx.x; float gv=g[i]; h[i]=f2bf((gv>0?gv:0.f)*u[i]); }
__global__ void k_res2(const float* x1, const float* a, const float* g, u16* out){ int i=blockIdx.x*blockDim.x+threadIdx.x; out[i]=f2bf(x1[i]+g[i%D]*a[i]); }

extern "C" void kernel_launch(void* const* d_in, const int* in_sizes, int n_in, void* d_out, int out_size, void* d_ws, size_t ws_size, hipStream_t stream) {
  const u16* x=(const u16*)d_in[0]; const u16* wqkv=(const u16*)d_in[1]; const u16* wphi=(const u16*)d_in[2]; const u16* wo=(const u16*)d_in[3];
  const u16* wg=(const u16*)d_in[5]; const u16* wu=(const u16*)d_in[6]; const u16* wd=(const u16*)d_in[7]; const u16* c=(const u16*)d_in[8]; const u16* wmod=(const u16*)d_in[9];
  char* w=(char*)d_ws; size_t off=0; auto alloc=[&](size_t b){ void* p=w+off; off+=(b+255)/256*256; return p; };
  float* mod=(float*)alloc(6*D*4); u16* h1=(u16*)alloc(S*D*2); float* qkv=(float*)alloc((size_t)S*3*D*4); float* phi=(float*)alloc((size_t)2*S*H*32*4);
  float* kv=(float*)alloc(H*544*4); u16* o=(u16*)alloc(S*D*2); float* a=(float*)alloc(S*D*4); float* x1=(float*)alloc(S*D*4); u16* h2=(u16*)alloc(S*D*2);
  float* g=(float*)alloc((size_t)S*F*4); float* u=(float*)alloc((size_t)S*F*4); u16* hh=(u16*)alloc((size_t)S*F*2); float* dn=(float*)alloc(S*D*4);
  k_mod<<<12,256,0,stream>>>(c,wmod,mod);
  k_ln<<<S,512,0,stream>>>(x,nullptr,mod,mod+D,h1);
  k_gemm<<<dim3(3*D/16,S/16),256,0,stream>>>(h1,wqkv,qkv,S,3*D,D);
  k_phi<<<2*S*H/256,256,0,stream>>>(qkv,wphi,phi);
  k_kv<<<H,576,0,stream>>>(qkv,phi,kv);
  k_o<<<S*D/256,256,0,stream>>>(phi,kv,o);
  k_gemm<<<dim3(D/16,S/16),256,0,stream>>>(o,wo,a,S,D,D);
  k_res1<<<S*D/256,256,0,stream>>>(x,a,mod+2*D,x1);
  k_ln<<<S,512,0,stream>>>(nullptr,x1,mod+3*D,mod+4*D,h2);
  k_gemm<<<dim3(F/16,S/16),256,0,stream>>>(h2,wg,g,S,F,D);
  k_gemm<<<dim3(F/16,S/16),256,0,stream>>>(h2,wu,u,S,F,D);
  k_glu<<<S*F/256,256,0,stream>>>(g,u,hh);
  k_gemm<<<dim3(D/16,S/16),256,0,stream>>>(hh,wd,dn,S,D,F);
  k_res2<<<S*D/256,256,0,stream>>>(x1,dn,mod+5*D,(u16*)d_out);
}
```

```cpp
#include <hip/hip_runtime.h>
#include <hip/hip_bf16.h>

struct Srd { const void* p; unsigned n; unsigned f; };
struct Args { Srd t[11]; };

__global__ void __launch_bounds__(256, 1) fused_block(Args args) {
#if defined(__HIP_DEVICE_COMPILE__)
  __shared__ __attribute__((aligned(16))) char lds[163840];
  asm volatile(
".Lk_start:\n"
" s_getpc_b64 s[50:51]\n"
" s_load_dwordx16 s[4:19], s[0:1], 0x0\n"
" s_load_dwordx16 s[20:35], s[0:1], 0x40\n"
" s_load_dwordx4 s[36:39], s[0:1], 0x80\n"
" v_lshrrev_b32 v1, 6, v0\n"
" v_and_b32 v0, 63, v0\n"
" v_readfirstlane_b32 s3, v1\n"
" s_lshr_b32 s75, s2, 3\n"
" s_sub_u32 s66, s75, 24\n"
" s_cmp_lt_u32 s66, 5\n"
" s_cselect_b32 s65, s3, 0\n"
" s_cmp_eq_u32 s65, 3\n"
" s_cbranch_scc0 .LL_nopf\n"
" v_lshlrev_b32 v5, 7, v0\n"
" v_lshl_add_u32 v5, s66, 13, v5\n"
" v_min_u32 v5, .Lk_end-.Lk_start-8, v5\n"
" global_load_dword v254, v5, s[50:51]\n"
".LL_nopf:\n"
" v_lshrrev_b32 v2, 4, v0\n"
" v_and_b32 v3, 15, v0\n"
" s_bfe_u32 s57, s2, 65538\n"
" s_bfe_u32 s65, s2, 196613\n"
" s_lshl3_add_u32 s57, s57, s65\n"
" s_bfe_u32 s65, s2, 65536\n"
" s_lshl1_add_u32 s57, s57, s65\n"
" s_bfe_u32 s60, s2, 65537\n"
" s_lshl_b32 s60, s60, 1\n"
" v_xor_b32 v12, v3, v2\n"
" v_lshlrev_b32 v12, 4, v12\n"
" v_lshl_add_u32 v8, v2, 10, v12\n"
" v_xor_b32 v9, 64, v8\n"
" v_add_u32 v9, 3072, v9\n"
" v_xor_b32 v10, 128, v8\n"
" v_add_u32 v10, 6144, v10\n"
" v_xor_b32 v11, 192, v8\n"
" v_add_u32 v11, 9216, v11\n"
" s_lshl_b32 s70, s57, 8\n"
" s_add_u32 s70, s70, s3\n"
" s_lshl_b32 s70, s70, 8\n"
" s_lshl_b32 s69, s60, 14\n"
" s_mov_b32 s85, s2\n"
" s_cmp_lt_u32 s85, 192\n"
" s_cbranch_scc0 .LL_nomod1\n"
" s_and_b32 s65, s3, 1\n"
" s_lshl_b32 s65, s65, 2\n"
" v_bfe_u32 v4, v0, 4, 1\n"
" v_lshl_or_b32 v4, v4, 1, s65\n"
" v_and_b32 v6, 7, v0\n"
" v_xor_b32 v4, v4, v6\n"
" v_lshlrev_b32_e64 v4, 4, v4\n"
" v_lshrrev_b32 v5, 3, v0\n"
" s_lshr_b32 s67, s85, 2\n"
" s_and_b32 s68, s85, 3\n"
" s_lshl4_add_u32 s65, s68, s3\n"
" s_mul_i32 s65, s65, 49152\n"
" s_lshl_b32 s66, s67, 7\n"
" s_add_u32 s90, s65, s66\n"
" v_mul_u32_u24 v192, 6144, v5\n"
" v_add_u32 v192, v192, v4\n"
" s_lshl_b32 s66, s3, 12\n"
" s_lshl2_add_u32 s65, s68, s3\n"
" s_lshl_b32 s65, s65, 6\n"
" v_and_b32 v193, 31, v0\n"
" v_lshl_add_u32 v193, v193, 1, s65\n"
" s_add_u32 m0, s66, 131072\n"
" s_waitcnt lgkmcnt(0)\n"
" buffer_load_ushort v194, v193, s[8:11], 0 offen\n"
" buffer_load_dwordx4 v192, s[4:7], s90 offen offset:0 nt lds\n"
" s_add_u32 s90, s90, 195584\n"
" buffer_load_dwordx4 v192, s[4:7], s90 offen offset:1024 nt lds\n"
" s_add_u32 s90, s90, 195584\n"
" buffer_load_dwordx4 v192, s[4:7], s90 offen offset:2048 nt lds\n"
" s_add_u32 s90, s90, 195584\n"
" buffer_load_dwordx4 v192, s[4:7], s90 offen offset:3072 nt lds\n"
" s_mul_i32 s65, s68, 48\n"
" s_add_u32 s65, s65, s67\n"
" s_lshl2_add_u32 s65, s65, s3\n"
" s_lshl_b32 s65, s65, 6\n"
" s_add_u32 s91, s65, 8192\n"
".LL_nomod1:\n"
" s_waitcnt lgkmcnt(0)\n"
" s_and_b32 s69, s69, 0xffff\n"
" s_add_u32 s51, s70, s69\n"
" s_lshl_b32 m0, s3, 14\n"
" s_add_u32 s69, s69, 16384\n"
" buffer_load_dwordx4 v8, s[12:15], s51 offen offset:0 lds\n"
" buffer_load_dwordx4 v9, s[12:15], s51 offen offset:1024 lds\n"
" buffer_load_dwordx4 v10, s[12:15], s51 offen offset:2048 lds\n"
" buffer_load_dwordx4 v11, s[12:15], s51 offen offset:3072 lds\n"
" s_load_dwordx2 s[48:49], s[16:17], 0\n"
" s_load_dwordx8 s[4:11], s[0:1], 0x90\n"
" s_and_b32 s69, s69, 0xffff\n"
" s_add_u32 s51, s70, s69\n"
" s_add_u32 m0, m0, 4096\n"
" s_add_u32 s69, s69, 16384\n"
" buffer_load_dwordx4 v8, s[12:15], s51 offen offset:0 lds\n"
" buffer_load_dwordx4 v9, s[12:15], s51 offen offset:1024 lds\n"
" s_and_b32 s65, s3, 1\n"
" s_lshl_b32 s65, s65, 2\n"
" v_bfe_u32 v4, v0, 4, 1\n"
" v_lshl_or_b32 v4, v4, 1, s65\n"
" v_and_b32 v6, 7, v0\n"
" v_xor_b32 v4, v4, v6\n"
" v_lshlrev_b32 v4, 4, v4\n"
" v_lshrrev_b32 v5, 3, v0\n"
" s_bfe_u32 s63, s2, 131072\n"
" s_bfe_u32 s65, s2, 131075\n"
" s_lshl2_add_u32 s63, s63, s65\n"
" s_bfe_u32 s58, s2, 65537\n"
" s_bfe_u32 s65, s2, 131075\n"
" s_lshl2_add_u32 s58, s58, s65\n"
" s_lshr_b32 s62, s57, 1\n"
" s_bfe_u32 s71, s2, 196613\n"
" s_lshl_b32 s66, s58, 7\n"
" s_mov_b32 s64, s71\n"
" v_lshlrev_b32 v1, 4, v0\n"
" s_mov_b32 s73, 0xffff0000\n"
" s_movk_i32 s74, 0xfff\n"
" s_mov_b32 s96, 0\n"
" s_mov_b32 s76, 0x3fb8aa3b\n"
" s_mov_b32 s77, s76\n"
" v_bfe_u32 v198, v0, 3, 2\n"
" v_xor_b32 v198, s3, v198\n"
" v_and_b32 v178, 3, v0\n"
" v_lshl_add_u32 v178, v198, 2, v178\n"
" v_and_b32 v198, 12, v0\n"
" v_lshl_add_u32 v178, v198, 2, v178\n"
" v_lshlrev_b32 v178, 3, v178\n"
" s_bfe_u32 s65, s2, 65538\n"
" s_bfe_u32 s67, s2, 65536\n"
" s_lshl1_add_u32 s65, s65, s67\n"
" s_lshl_b32 s65, s65, 2\n"
" s_lshl_b64 s[92:93], 15, s65\n"
" s_lshl2_add_u32 s65, s71, s3\n"
" v_lshl_add_u32 v198, s65, 4, v3\n"
" v_lshl_add_u32 v186, v198, 10, s66\n"
" v_mul_u32_u24 v198, 3072, v198\n"
" v_add_u32 v202, s66, v198\n"
" v_lshl_add_u32 v205, v2, 12, v178\n"
" v_add_u32 v176, 65536, v205\n"
" v_lshl_add_u32 v178, v2, 11, v178\n"
" v_add_u32 v201, 65536, v1\n"
" s_lshl2_add_u32 s59, s58, s3\n"
" s_mul_i32 s65, s3, 24576\n"
" s_add_u32 s65, s65, s66\n"
" v_mul_u32_u24 v177, 3072, v5\n"
" v_add3_u32 v177, v177, v4, s65\n"
" v_add_u32 v6, 1024, v177\n"
" v_add_u32 v7, 97280, v177\n"
" s_and_b32 s67, s2, 4\n"
" s_lshl_b32 s68, s3, 1\n"
" s_xor_b32 s66, s68, s67\n"
" s_and_b32 s66, s66, 4\n"
" s_sub_u32 s68, s68, s64\n"
" s_add_u32 s65, s68, 8\n"
" s_and_b32 s65, s65, 3\n"
" s_or_b32 s65, s65, s66\n"
" v_lshl_add_u32 v206, s65, 13, v201\n"
" s_add_u32 s65, s68, 9\n"
" s_and_b32 s65, s65, 3\n"
" s_or_b32 s65, s65, s66\n"
" v_lshl_add_u32 v207, s65, 13, v201\n"
" s_add_u32 s65, s64, 0\n"
" s_and_b32 s65, s65, 3\n"
" s_or_b32 s65, s65, s67\n"
" s_mul_i32 s40, s65, 196608\n"
" s_xor_b32 s65, s65, 4\n"
" s_mul_i32 s44, s65, 196608\n"
" s_add_u32 s65, s64, 1\n"
" s_and_b32 s65, s65, 3\n"
" s_or_b32 s65, s65, s67\n"
" s_mul_i32 s41, s65, 196608\n"
" s_xor_b32 s65, s65, 4\n"
" s_mul_i32 s45, s65, 196608\n"
" s_add_u32 s65, s64, 2\n"
" s_and_b32 s65, s65, 3\n"
" s_or_b32 s65, s65, s67\n"
" s_mul_i32 s42, s65, 196608\n"
" s_xor_b32 s65, s65, 4\n"
" s_mul_i32 s46, s65, 196608\n"
" s_add_u32 s65, s64, 3\n"
" s_and_b32 s65, s65, 3\n"
" s_or_b32 s65, s65, s67\n"
" s_mul_i32 s43, s65, 196608\n"
" s_xor_b32 s65, s65, 4\n"
" s_mul_i32 s47, s65, 196608\n"
" v_lshl_add_u32 v12, v3, 8, v12\n"
" v_lshl_add_u32 v12, s3, 14, v12\n"
" v_xor_b32 v13, 64, v12\n"
" v_xor_b32 v14, 128, v12\n"
" v_xor_b32 v15, 192, v12\n"
" v_mov_b32 v208, 0x3b003b00\n"
" v_mov_b32 v209, 0x3b003b00\n"
" v_mov_b32 v210, 0x3b003b00\n"
" v_mov_b32 v211, 0x3b003b00\n"
" v_mov_b32 v204, 0x3727c5ac\n"
" s_mov_b32 s82, 0x3b000000\n"
" s_mov_b32 s86, 0xaaaaaaaa\n"
" s_mov_b32 s87, s86\n"
" s_mov_b32 s88, 0xcccccccc\n"
" s_mov_b32 s89, s88\n"
" v_lshlrev_b32 v180, 5, v3\n"
" v_add_u32 v180, 151552, v180\n"
" v_lshl_add_u32 v179, s3, 3, v180\n"
" v_lshrrev_b32 v187, 5, v0\n"
" v_and_b32 v181, 0x1ff, v1\n"
" v_lshl_add_u32 v181, v187, 11, v181\n"
" v_lshl_add_u32 v181, s3, 9, v181\n"
" v_cvt_f32_u32 v187, v187\n"
" s_lshl_b32 s100, s3, 10\n"
" s_add_u32 s100, s100, 154112\n"
" v_add_u32 v182, s100, v1\n"
" v_lshl_add_u32 v183, v2, 5, s100\n"
" v_and_b32 v198, 3, v0\n"
" v_lshl_add_u32 v183, v198, 2, v183\n"
" v_lshlrev_b32 v198, 4, v198\n"
" s_mov_b64 s[66:67], 0xffff\n"
" v_lshlrev_b64 v[236:237], v198, s[66:67]\n"
" v_lshrrev_b32 v198, 5, v0\n"
" v_lshl_add_u32 v242, v198, 8, s100\n"
" v_and_b32 v198, 12, v0\n"
" v_lshl_add_u32 v242, v198, 3, v242\n"
" v_and_b32 v198, 16, v0\n"
" v_and_b32 v199, 2, v0\n"
" v_lshl_add_u32 v198, v199, 2, v198\n"
" v_add_u32 v242, v242, v198\n"
" v_bfe_i32 v198, v0, 2, 1\n"
" v_and_b32 v243, 0x80008000, v198\n"
" s_lshl_b32 s72, s3, 12\n"
" s_add_u32 s72, s72, 131072\n"
" s_cmp_lt_u32 s75, 22\n"
" s_cbranch_scc0 .LL_ptA\n"
" s_and_b32 s65, s2, 3\n"
" s_lshl2_add_u32 s65, s65, s3\n"
" s_bfe_u32 s66, s2, 65538\n"
" s_mul_i32 s66, s66, 22\n"
" s_add_u32 s66, s66, s75\n"
" s_lshl_b32 s66, s66, 6\n"
" s_add_u32 s67, s65, s66\n"
" s_lshl_b32 s67, s67, 10\n"
" s_add_u32 s50, s67, 6815744\n"
" s_mul_i32 s65, s65, 88064\n"
" s_add_u32 s84, s65, s66\n"
" s_cmp_lt_u32 s66, 2752\n"
" s_cselect_b32 s84, s84, 1073741824\n"
" s_cselect_b32 s50, s50, 1073741824\n"
" s_mov_b32 s95, s84\n"
" s_movk_i32 s64, 0x4000\n"
" v_lshrrev_b32 v198, 2, v0\n"
" v_mul_u32_u24 v198, 2752, v198\n"
" v_and_b32 v199, 0x30, v1\n"
" v_add_u32 v184, v198, v199\n"
" v_add_u32 v203, 43008, v184\n"
" v_and_b32 v185, 0x3c0, v1\n"
" v_lshrrev_b32 v199, 1, v199\n"
" v_add_u32 v185, v185, v199\n"
" s_movk_i32 s65, 0x800\n"
" s_movk_i32 s66, 32\n"
" s_mov_b32 s97, 0x33333333\n"
" s_mov_b32 s83, 0xcccccccc\n"
".LL_ptA:\n"
" s_cmp_ge_u32 s75, 22\n"
" s_cbranch_scc0 .LL_ptB\n"
" s_and_b32 s65, s2, 7\n"
" s_sub_u32 s66, s75, 22\n"
" s_lshl2_add_u32 s66, s66, s3\n"
" s_mul_i32 s67, s65, 40\n"
" s_add_u32 s67, s67, s66\n"
" s_lshl_b32 s67, s67, 12\n"
" s_add_u32 s50, s67, 9633792\n"
" s_cmp_lt_u32 s66, 32\n"
" s_cselect_b64 vcc, -1, 0\n"
" s_lshl_b32 s66, s66, 15\n"
" s_lshl_b32 s65, s65, 7\n"
" s_add_u32 s84, s65, s66\n"
" s_add_u32 s95, s84, 16384\n"
" s_movk_i32 s64, 0x400\n"
" s_mov_b64 s[24:25], s[32:33]\n"
" s_mov_b64 s[28:29], s[32:33]\n"
" v_and_b32 v184, 0x70, v1\n"
" v_lshrrev_b32_e64 v198, 3, v0\n"
" v_lshl_add_u32 v184, v198, 10, v184\n"
" v_add_u32 v203, 7168, v184\n"
" v_bfe_u32 v198, v0, 2, 1\n"
" v_bfe_u32 v199, v0, 4, 1\n"
" v_lshl_add_u32 v198, v199, 1, v198\n"
" v_bfe_u32 v199, v0, 3, 1\n"
" v_lshl_add_u32 v198, v199, 2, v198\n"
" v_bfe_u32 v199, v0, 2, 3\n"
" v_cndmask_b32 v198, v198, v199, vcc\n"
" v_lshrrev_b32 v199, 5, v0\n"
" v_lshl_add_u32 v198, v199, 4, v198\n"
" v_and_b32 v185, 3, v0\n"
" v_lshlrev_b32 v185, 3, v185\n"
" v_lshl_add_u32 v185, v198, 7, v185\n"
" s_movk_i32 s65, 32\n"
" s_movk_i32 s66, 64\n"
" s_mov_b32 s97, -1\n"
" s_mov_b32 s83, -1\n"
".LL_ptB:\n"
" v_add_u32 v248, s72, v185\n"
" v_add_u32 v249, s65, v248\n"
" v_add_u32 v250, s66, v248\n"
" v_add_u32 v251, s66, v249\n"
" s_lshl_b32 s66, s58, 7\n"
" s_lshl3_add_u32 s65, s3, s71\n"
" s_add_u32 s65, s65, 320\n"
" v_lshl_add_u32 v198, s65, 2, v3\n"
" v_lshl_add_u32 v185, v198, 10, s66\n"
" s_waitcnt lgkmcnt(0)\n"
" s_add_u32 s48, s48, 0x01001001\n"
" s_addc_u32 s49, s49, 0x10\n"
" s_and_b32 s52, s48, 0xfff\n"
" s_bfe_u32 s53, s48, 786444\n"
" s_lshr_b64 s[66:67], s[48:49], 24\n"
" s_and_b32 s54, s66, 0xfff\n"
" s_bfe_u32 s55, s66, 786444\n"
" v_mov_b32 v244, s52\n"
" v_mov_b32 v245, s53\n"
" v_mov_b32 v246, s54\n"
" v_mov_b32 v247, s55\n"
" v_and_b32 v198, 1, v2\n"
" v_add_u32 v198, s60, v198\n"
" v_and_b32 v198, 3, v198\n"
" v_lshrrev_b32 v199, 1, v2\n"
" v_lshl_add_u32 v241, v199, 4, v3\n"
" v_lshlrev_b32 v240, 2, v241\n"
" v_lshlrev_b32 v241, 4, v241\n"
" v_lshl_add_u32 v241, v198, 14, v241\n"
" v_xor_b32 v239, 0x8000, v241\n"
" v_mov_b32 v225, 0x3f803f80\n"
" v_cndmask_b32 v228, v225, 0, s[86:87]\n"
" v_cndmask_b32 v232, 0, v225, s[86:87]\n"
" v_mov_b32 v229, v228\n"
" v_mov_b32 v233, v232\n"
" v_mov_b32 v230, v228\n"
" v_mov_b32 v234, v232\n"
" v_mov_b32 v231, v228\n"
" v_mov_b32 v235, v232\n"
" v_lshrrev_b32 v198, 2, v0\n"
" v_and_b32 v199, 0x30, v1\n"
" v_lshl_add_u32 v188, v198, 11, v199\n"
" v_lshlrev_b32 v189, 5, v2\n"
" v_lshl_add_u32 v189, v3, 4, v189\n"
" s_mul_i32 s65, s63, 3\n"
" s_lshr_b32 s68, s63, 2\n"
" s_sub_u32 s65, s65, s68\n"
" s_add_u32 s65, s65, s3\n"
" s_add_u32 s65, s65, 128\n"
" s_lshl2_add_u32 s67, s63, s3\n"
" s_lshl_b32 s67, s67, 1\n"
" s_lshl_b32 s68, s67, 14\n"
" s_add_u32 s80, s68, 6815744\n"
" s_lshl_b32 s68, s65, 14\n"
" s_add_u32 s81, s68, 6815744\n"
" s_mul_i32 s68, s62, 1376\n"
" s_add_u32 s94, s68, s67\n"
" s_lshl_b32 s94, s94, 8\n"
" s_add_u32 s94, s94, 11042816\n"
" s_add_u32 s56, s68, s65\n"
" s_lshl_b32 s56, s56, 8\n"
" s_add_u32 s56, s56, 11042816\n"
" s_and_b32 s68, s63, 3\n"
" s_cmp_eq_u32 s68, 3\n"
" s_cselect_b32 s68, 2, 3\n"
" s_cmp_lt_u32 s3, s68\n"
" s_cselect_b32 s81, s81, 1073741824\n"
" s_cselect_b32 s56, s56, 1073741824\n"
" s_lshr_b32 s68, s67, 2\n"
" s_lshr_b32 s65, s65, 2\n"
" s_min_u32 s65, s65, 42\n"
" s_lshl_b32 s65, s65, 3\n"
" s_sub_u32 s65, s65, 176\n"
" s_lshl_b32 s67, s68, 3\n"
" s_cmp_lt_u32 s68, 22\n"
" s_cselect_b32 s68, 0, 172\n"
" s_sub_u32 s67, s67, s68\n"
" v_cmp_gt_u32 vcc, 4, v3\n"
" v_add_u32 v198, s65, v3\n"
" v_add_u32 v199, s67, v3\n"
" s_add_u32 s68, s58, 176\n"
" v_cndmask_b32 v198, v198, v199, vcc\n"
" v_cmp_eq_u32 vcc, 1, v2\n"
" v_lshl_add_u32 v199, v3, 3, s68\n"
" s_and_b32 s68, s2, 228\n"
" v_and_b32 v200, 3, v3\n"
" v_cndmask_b32 v198, v198, v199, vcc\n"
" s_and_b32 s65, s3, 2\n"
" s_add_u32 s65, s65, s3\n"
" s_and_b32 s67, s2, 26\n"
" s_add_u32 s65, s65, s67\n"
" v_cmp_eq_u32 vcc, 2, v2\n"
" v_lshl_add_u32 v199, v3, 5, s65\n"
" s_nop 0\n"
" v_cndmask_b32 v198, v198, v199, vcc\n"
" v_lshl_add_u32 v200, v200, 3, s68\n"
" v_cmp_eq_u32 vcc, 3, v2\n"
" v_lshrrev_b32 v199, 2, v3\n"
" v_add_u32 v199, v199, v200\n"
" v_cndmask_b32 v198, v198, v199, vcc\n"
" v_lshlrev_b32 v198, 7, v198\n"
" v_add_u32 v238, 425984, v198\n"
" v_and_b32 v198, 1, v2\n"
" v_lshrrev_b32 v199, 1, v2\n"
" v_lshl_add_u32 v198, v198, 1, v199\n"
" v_lshlrev_b32 v190, 4, v3\n"
" v_lshl_add_u32 v190, v198, 2, v190\n"
" v_mul_u32_u24 v198, 12, v198\n"
" v_lshrrev_b64 v[198:199], v198, s[48:49]\n"
" v_mov_b32 v191, v198\n"
" buffer_load_dwordx4 v10, s[12:15], s51 offen offset:2048 lds\n"
" buffer_load_dwordx4 v11, s[12:15], s51 offen offset:3072 lds\n"
" s_cmp_lt_u32 s85, 192\n"
" s_cbranch_scc0 .LL_nomod2\n"
" v_add_u32 v195, 65536, v176\n"
" v_lshlrev_b32 v197, 4, v2\n"
" v_add_u32 v196, 153600, v197\n"
" v_and_b32 v193, 0xff, v193\n"
" v_add_u32 v193, 153600, v193\n"
" s_waitcnt vmcnt(8)\n"
" v_lshlrev_b32 v194, 16, v194\n"
" v_mul_f32 v198, s76, v194\n"
" v_exp_f32 v198, -v198\n"
" s_nop 0\n"
" v_add_f32 v198, 1.0, v198\n"
" v_rcp_f32 v198, v198\n"
" s_nop 0\n"
" v_mul_f32_e64 v198, v194, v198\n"
" v_cvt_pk_bf16_f32 v198, v198, v198\n"
" ds_write_b16 v193, v198\n"
" s_waitcnt lgkmcnt(0)\n"
" s_barrier\n"
" ds_read_b128 v[144:147], v196 offset:0\n"
" ds_read_b64_tr_b16 v[148:149], v195 offset:0\n"
" ds_read_b64_tr_b16 v[150:151], v195 offset:512\n"
" ds_read_b128 v[152:155], v196 offset:64\n"
" ds_read_b64_tr_b16 v[156:157], v195 offset:1024\n"
" ds_read_b64_tr_b16 v[158:159], v195 offset:1536\n"
" ds_read_b128 v[160:163], v196 offset:128\n"
" ds_read_b64_tr_b16 v[164:165], v195 offset:2048\n"
" ds_read_b64_tr_b16 v[166:167], v195 offset:2560\n"
" ds_read_b128 v[168:171], v196 offset:192\n"
" ds_read_b64_tr_b16 v[172:173], v195 offset:3072\n"
" ds_read_b64_tr_b16 v[174:175], v195 offset:3584\n"
".LL_nomod2:\n"
" s_and_b32 s69, s69, 0xffff\n"
" s_add_u32 s51, s70, s69\n"
" s_nop 0\n"
" s_add_u32 m0, m0, 4096\n"
" s_add_u32 s69, s69, 16384\n"
" buffer_load_dwordx4 v8, s[12:15], s51 offen offset:0 lds\n"
" buffer_load_dwordx4 v9, s[12:15], s51 offen offset:1024 lds\n"
" buffer_load_dwordx4 v10, s[12:15], s51 offen offset:2048 lds\n"
" buffer_load_dwordx4 v11, s[12:15], s51 offen offset:3072 lds\n"
" s_and_b32 s69, s69, 0xffff\n"
" s_add_u32 s51, s70, s69\n"
" s_add_u32 m0, m0, 4096\n"
" s_nop 0\n"
" buffer_load_dwordx4 v8, s[12:15], s51 offen offset:0 lds\n"
" buffer_load_dwordx4 v9, s[12:15], s51 offen offset:1024 lds\n"
" buffer_load_dwordx4 v10, s[12:15], s51 offen offset:2048 lds\n"
" buffer_load_dwordx4 v11, s[12:15], s51 offen offset:3072 lds\n"
" s_cmp_lt_u32 s85, 192\n"
" s_cbranch_scc0 .LL_nomod2b\n"
" s_waitcnt lgkmcnt(0)\n"
" v_mfma_f32_16x16x32_bf16 v[212:215], v[148:151], v[144:147], 0\n"
" v_mfma_f32_16x16x32_bf16 v[212:215], v[156:159], v[152:155], v[212:215]\n"
" v_mfma_f32_16x16x32_bf16 v[212:215], v[164:167], v[160:163], v[212:215]\n"
" v_mfma_f32_16x16x32_bf16 v[212:215], v[172:175], v[168:171], v[212:215]\n"
".LL_nomod2b:\n"
" s_waitcnt vmcnt(12)\n"
" ds_read_b128 v[18:21], v12 offset:0\n"
" ds_read_b128 v[24:27], v13 offset:0\n"
" ds_read_b128 v[30:33], v14 offset:0\n"
" ds_read_b128 v[36:39], v15 offset:0\n"
" s_cmp_lt_u32 s85, 192\n"
" s_cbranch_scc0 .LL_nomod2c\n"
" s_mov_b32 exec_lo, 0x00010001\n"
" s_mov_b32 exec_hi, 0x00010001\n"
" v_bfi_b32 v212, s74, v244, v212\n"
" v_bfi_b32 v213, s74, v245, v213\n"
" v_bfi_b32 v214, s74, v246, v214\n"
" v_bfi_b32 v215, s74, v247, v215\n"
" buffer_store_dwordx4 v[212:215], v197, s[16:19], s91 offen sc1\n"
" s_mov_b64 exec, -1\n"
".LL_nomod2c:\n"
" s_mov_b32 m0, s100\n"
" s_lshl_b32 s65, s59, 9\n"
" s_waitcnt vmcnt(8)\n"
" ds_read_b128 v[44:47], v12 offset:4096\n"
" ds_read_b128 v[50:53], v13 offset:4096\n"
" ds_read_b128 v[56:59], v14 offset:4096\n"
" ds_read_b128 v[62:65], v15 offset:4096\n"
" s_waitcnt lgkmcnt(7)\n"
" v_mfma_f32_16x16x32_bf16 v[120:123], v[208:211], v[18:21], 0\n"
" buffer_load_dwordx4 v1, s[36:39], s65 offen lds\n"
" s_add_u32 m0, s72, 4294901760\n"
" v_mfma_f32_16x16x32_bf16 v[124:127], v[18:21], v[18:21], 0\n"
" s_waitcnt lgkmcnt(6)\n"
" v_mfma_f32_16x16x32_bf16 v[120:123], v[208:211], v[24:27], v[120:123]\n"
" buffer_load_dwordx4 v6, s[20:23], s40 offen offset:0 lds\n"
" v_mfma_f32_16x16x32_bf16 v[124:127], v[24:27], v[24:27], v[124:127]\n"
" s_waitcnt lgkmcnt(5)\n"
" v_mfma_f32_16x16x32_bf16 v[120:123], v[208:211], v[30:33], v[120:123]\n"
" v_mfma_f32_16x16x32_bf16 v[124:127], v[30:33], v[30:33], v[124:127]\n"
" buffer_load_dwordx4 v6, s[20:23], s40 offen offset:1024 lds\n"
" s_waitcnt lgkmcnt(4)\n"
" v_mfma_f32_16x16x32_bf16 v[120:123], v[208:211], v[36:39], v[120:123]\n"
" v_mfma_f32_16x16x32_bf16 v[124:127], v[36:39], v[36:39], v[124:127]\n"
" s_waitcnt vmcnt(7)\n"
" ds_read_b128 v[70:73], v12 offset:8192\n"
" ds_read_b128 v[76:79], v13 offset:8192\n"
" ds_read_b128 v[82:85], v14 offset:8192\n"
" ds_read_b128 v[88:91], v15 offset:8192\n"
" s_waitcnt lgkmcnt(7)\n"
" v_mfma_f32_16x16x32_bf16 v[128:131], v[208:211], v[44:47], 0\n"
" buffer_load_dwordx4 v7, s[20:23], s40 offen offset:2048 lds\n"
" v_mfma_f32_16x16x32_bf16 v[132:135], v[44:47], v[44:47], 0\n"
" s_waitcnt lgkmcnt(6)\n"
" v_mfma_f32_16x16x32_bf16 v[128:131], v[208:211], v[50:53], v[128:131]\n"
" buffer_load_dwordx4 v7, s[20:23], s40 offen offset:3072 lds\n"
" s_add_u32 m0, s72, 4294918144\n"
" v_mfma_f32_16x16x32_bf16 v[132:135], v[50:53], v[50:53], v[132:135]\n"
" s_waitcnt lgkmcnt(5)\n"
" v_mfma_f32_16x16x32_bf16 v[128:131], v[208:211], v[56:59], v[128:131]\n"
" v_cndmask_b32 v124, v124, v125, s[86:87]\n"
" v_mfma_f32_16x16x32_bf16 v[132:135], v[56:59], v[56:59], v[132:135]\n"
" buffer_load_dwordx4 v6, s[20:23], s41 offen offset:0 lds\n"
" v_cndmask_b32 v126, v126, v127, s[86:87]\n"
" s_waitcnt lgkmcnt(4)\n"
" v_mfma_f32_16x16x32_bf16 v[128:131], v[208:211], v[62:65], v[128:131]\n"
" v_cndmask_b32 v121, v124, v126, s[88:89]\n"
" v_mfma_f32_16x16x32_bf16 v[132:135], v[62:65], v[62:65], v[132:135]\n"
" s_waitcnt vmcnt(6)\n"
" ds_read_b128 v[96:99], v12 offset:12288\n"
" ds_read_b128 v[102:105], v13 offset:12288\n"
" ds_read_b128 v[108:111], v14 offset:12288\n"
" ds_read_b128 v[114:117], v15 offset:12288\n"
" s_waitcnt lgkmcnt(7)\n"
" v_mfma_f32_16x16x32_bf16 v[136:139], v[208:211], v[70:73], 0\n"
" buffer_load_dwordx4 v6, s[20:23], s41 offen offset:1024 lds\n"
" v_mfma_f32_16x16x32_bf16 v[140:143], v[70:73], v[70:73], 0\n"
" s_waitcnt lgkmcnt(6)\n"
" v_mfma_f32_16x16x32_bf16 v[136:139], v[208:211], v[76:79], v[136:139]\n"
" buffer_load_dwordx4 v7, s[20:23], s41 offen offset:2048 lds\n"
" v_mfma_f32_16x16x32_bf16 v[140:143], v[76:79], v[76:79], v[140:143]\n"
" s_waitcnt lgkmcnt(5)\n"
" v_mfma_f32_16x16x32_bf16 v[136:139], v[208:211], v[82:85], v[136:139]\n"
" v_cndmask_b32 v132, v132, v133, s[86:87]\n"
" v_mfma_f32_16x16x32_bf16 v[140:143], v[82:85], v[82:85], v[140:143]\n"
" buffer_load_dwordx4 v7, s[20:23], s41 offen offset:3072 lds\n"
" s_add_u32 m0, s72, 4294934528\n"
" v_cndmask_b32 v134, v134, v135, s[86:87]\n"
" s_waitcnt lgkmcnt(4)\n"
" v_mfma_f32_16x16x32_bf16 v[136:139], v[208:211], v[88:91], v[136:139]\n"
" v_cndmask_b32 v129, v132, v134, s[88:89]\n"
" v_mfma_f32_16x16x32_bf16 v[140:143], v[88:91], v[88:91], v[140:143]\n"
" s_waitcnt lgkmcnt(3)\n"
" v_mfma_f32_16x16x32_bf16 v[144:147], v[208:211], v[96:99], 0\n"
" buffer_load_dwordx4 v6, s[20:23], s42 offen offset:0 lds\n"
" v_mfma_f32_16x16x32_bf16 v[148:151], v[96:99], v[96:99], 0\n"
" s_waitcnt lgkmcnt(2)\n"
" v_mfma_f32_16x16x32_bf16 v[144:147], v[208:211], v[102:105], v[144:147]\n"
" buffer_load_dwordx4 v6, s[20:23], s42 offen offset:1024 lds\n"
" v_mfma_f32_16x16x32_bf16 v[148:151], v[102:105], v[102:105], v[148:151]\n"
" s_waitcnt lgkmcnt(1)\n"
" v_mfma_f32_16x16x32_bf16 v[144:147], v[208:211], v[108:111], v[144:147]\n"
" v_cndmask_b32 v140, v140, v141, s[86:87]\n"
" v_mfma_f32_16x16x32_bf16 v[148:151], v[108:111], v[108:111], v[148:151]\n"
" buffer_load_dwordx4 v7, s[20:23], s42 offen offset:2048 lds\n"
" v_cndmask_b32 v142, v142, v143, s[86:87]\n"
" s_waitcnt lgkmcnt(0)\n"
" v_mfma_f32_16x16x32_bf16 v[144:147], v[208:211], v[114:117], v[144:147]\n"
" v_cndmask_b32 v137, v140, v142, s[88:89]\n"
" v_mfma_f32_16x16x32_bf16 v[148:151], v[114:117], v[114:117], v[148:151]\n"
" s_mov_b32 exec_lo, 0x00f0000f\n"
" s_mov_b32 exec_hi, 0xf0000f00\n"
" ds_write_b64 v179, v[120:121] offset:0\n"
" ds_write_b64 v179, v[128:129] offset:512\n"
" ds_write_b64 v179, v[136:137] offset:1024\n"
" s_nop 2\n"
" v_cndmask_b32 v148, v148, v149, s[86:87]\n"
" v_cndmask_b32 v150, v150, v151, s[86:87]\n"
" v_cndmask_b32 v145, v148, v150, s[88:89]\n"
" ds_write_b64 v179, v[144:145] offset:1536\n"
" s_mov_b64 exec, -1\n"
" s_waitcnt lgkmcnt(0)\n"
" s_barrier\n"
" ds_read_b128 v[120:123], v180 offset:0\n"
" ds_read_b128 v[124:127], v180 offset:16\n"
" ds_read_b128 v[128:131], v180 offset:512\n"
" ds_read_b128 v[132:135], v180 offset:528\n"
" ds_read_b128 v[136:139], v180 offset:1024\n"
" ds_read_b128 v[140:143], v180 offset:1040\n"
" ds_read_b128 v[144:147], v180 offset:1536\n"
" ds_read_b128 v[148:151], v180 offset:1552\n"
" buffer_load_dwordx4 v7, s[20:23], s42 offen offset:3072 lds\n"
" s_add_u32 m0, s72, 4294950912\n"
" s_nop 0\n"
" buffer_load_dwordx4 v6, s[20:23], s43 offen offset:0 lds\n"
" s_waitcnt lgkmcnt(7)\n"
" v_pk_add_f32 v[120:121], v[120:121], v[122:123]\n"
" s_waitcnt lgkmcnt(5)\n"
" v_pk_add_f32 v[128:129], v[128:129], v[130:131]\n"
" s_waitcnt lgkmcnt(3)\n"
" v_pk_add_f32 v[136:137], v[136:137], v[138:139]\n"
" buffer_load_dwordx4 v6, s[20:23], s43 offen offset:1024 lds\n"
" s_waitcnt lgkmcnt(1)\n"
" v_pk_add_f32 v[144:145], v[144:145], v[146:147]\n"
" v_pk_add_f32 v[124:125], v[124:125], v[126:127]\n"
" v_pk_add_f32 v[132:133], v[132:133], v[134:135]\n"
" v_pk_add_f32 v[140:141], v[140:141], v[142:143]\n"
" s_waitcnt lgkmcnt(0)\n"
" v_pk_add_f32 v[148:149], v[148:149], v[150:151]\n"
" v_pk_add_f32 v[120:121], v[120:121], v[124:125]\n"
" v_pk_add_f32 v[128:129], v[128:129], v[132:133]\n"
" v_pk_add_f32 v[136:137], v[136:137], v[140:141]\n"
" v_pk_add_f32 v[144:145], v[144:145], v[148:149]\n"
" v_fma_f32 v121, v121, s82, v204\n"
" buffer_load_dwordx4 v7, s[20:23], s43 offen offset:2048 lds\n"
" v_fma_f32 v129, v129, s82, v204\n"
" v_fma_f32 v137, v137, s82, v204\n"
" v_fma_f32 v145, v145, s82, v204\n"
" v_fma_f32 v121, -v120, v120, v121\n"
" v_fma_f32 v129, -v128, v128, v129\n"
" v_fma_f32 v137, -v136, v136, v137\n"
" v_fma_f32 v145, -v144, v144, v145\n"
" v_max_f32 v121, v204, v121\n"
" v_max_f32 v129, v204, v129\n"
" v_max_f32 v137, v204, v137\n"
" v_max_f32 v145, v204, v145\n"
" buffer_load_dwordx4 v7, s[20:23], s43 offen offset:3072 lds\n"
" s_add_u32 m0, s72, 4294836224\n"
" v_rsq_f32 v208, v121\n"
" v_rsq_f32 v209, v129\n"
" v_rsq_f32 v210, v137\n"
" v_rsq_f32 v211, v145\n"
" v_and_b32 v122, s73, v120\n"
" v_and_b32 v130, s73, v128\n"
" v_and_b32 v138, s73, v136\n"
" v_and_b32 v146, s73, v144\n"
" v_mul_f32 v121, v121, v208\n"
" v_mul_f32 v129, v129, v209\n"
" v_mul_f32 v137, v137, v210\n"
" s_mov_b32 s67, 8192\n"
" buffer_load_dwordx4 v[212:215], v181, s[16:19], s67 offen sc1\n"
" v_mul_f32 v145, v145, v211\n"
" v_sub_f32 v120, v120, v122\n"
" v_sub_f32 v128, v128, v130\n"
" v_sub_f32 v136, v136, v138\n"
" v_sub_f32 v144, v144, v146\n"
" v_and_b32 v123, s73, v121\n"
" v_and_b32 v131, s73, v129\n"
" v_and_b32 v139, s73, v137\n"
" v_and_b32 v147, s73, v145\n"
" v_cvt_pk_bf16_f32 v16, -v122, -v120\n"
" v_cvt_pk_bf16_f32 v42, -v130, -v128\n"
" s_mov_b32 s67, 20480\n"
" buffer_load_dwordx4 v[216:219], v181, s[16:19], s67 offen sc1\n"
" v_cvt_pk_bf16_f32 v68, -v138, -v136\n"
" v_cvt_pk_bf16_f32 v94, -v146, -v144\n"
" v_sub_f32 v121, v121, v123\n"
" v_sub_f32 v129, v129, v131\n"
" v_sub_f32 v137, v137, v139\n"
" v_sub_f32 v145, v145, v147\n"
" v_cvt_pk_bf16_f32 v17, v123, v121\n"
" v_cvt_pk_bf16_f32 v43, v131, v129\n"
" v_cvt_pk_bf16_f32 v69, v139, v137\n"
" v_cvt_pk_bf16_f32 v95, v147, v145\n"
" v_mov_b64 v[22:23], v[16:17]\n"
" s_mov_b32 s67, 32768\n"
" buffer_load_dwordx4 v[220:223], v181, s[16:19], s67 offen sc1\n"
" v_mov_b64 v[48:49], v[42:43]\n"
" v_mov_b64 v[74:75], v[68:69]\n"
" v_mov_b64 v[100:101], v[94:95]\n"
" v_mov_b64 v[28:29], v[16:17]\n"
" v_mov_b64 v[54:55], v[42:43]\n"
" v_mov_b64 v[80:81], v[68:69]\n"
" v_mov_b64 v[106:107], v[94:95]\n"
" v_mov_b64 v[34:35], v[16:17]\n"
" v_mov_b64 v[60:61], v[42:43]\n"
" v_mov_b64 v[86:87], v[68:69]\n"
" v_mov_b64 v[112:113], v[94:95]\n"
" s_mov_b32 s67, 45056\n"
" buffer_load_dwordx4 v[224:227], v181, s[16:19], s67 offen sc1\n"
" v_mov_b64 v[40:41], v[16:17]\n"
" v_mov_b64 v[66:67], v[42:43]\n"
" v_mov_b64 v[92:93], v[68:69]\n"
" v_mov_b64 v[118:119], v[94:95]\n"
" v_mov_b64 v[144:145], 0\n"
" v_mov_b64 v[150:151], 0\n"
" v_mov_b64 v[152:153], 0\n"
" v_mov_b64 v[158:159], 0\n"
" v_mov_b64 v[160:161], 0\n"
" v_mov_b64 v[166:167], 0\n"
" v_mov_b64 v[168:169], 0\n"
" v_mov_b64 v[174:175], 0\n"
" s_waitcnt vmcnt(4)\n"
" s_barrier\n"
" ds_read_b64_tr_b16 v[12:13], v242\n"
" buffer_load_dwordx4 v6, s[20:23], s44 offen offset:0 lds\n"
" ds_read_b64_tr_b16 v[14:15], v242 offset:128\n"
" ds_read_b64_tr_b16 a[0:1], v176 offset:0\n"
" ds_read_b64_tr_b16 a[2:3], v176 offset:512\n"
" ds_read_b64_tr_b16 a[4:5], v176 offset:1024\n"
" ds_read_b64_tr_b16 a[6:7], v176 offset:1536\n"
" ds_read_b64_tr_b16 a[8:9], v176 offset:2048\n"
" ds_read_b64_tr_b16 a[10:11], v176 offset:2560\n"
" buffer_load_dwordx4 v6, s[20:23], s44 offen offset:1024 lds\n"
" ds_read_b64_tr_b16 a[12:13], v176 offset:3072\n"
" ds_read_b64_tr_b16 a[14:15], v176 offset:3584\n"
" ds_read_b64_tr_b16 a[16:17], v176 offset:16384\n"
" ds_read_b64_tr_b16 a[18:19], v176 offset:16896\n"
" ds_read_b64_tr_b16 a[20:21], v176 offset:17408\n"
" ds_read_b64_tr_b16 a[22:23], v176 offset:17920\n"
" ds_read_b64_tr_b16 a[24:25], v176 offset:18432\n"
" buffer_load_dwordx4 v7, s[20:23], s44 offen offset:2048 lds\n"
" ds_read_b64_tr_b16 a[26:27], v176 offset:18944\n"
" ds_read_b64_tr_b16 a[28:29], v176 offset:19456\n"
" ds_read_b64_tr_b16 a[30:31], v176 offset:19968\n"
" ds_read_b64_tr_b16 a[32:33], v176 offset:32768\n"
" ds_read_b64_tr_b16 a[34:35], v176 offset:33280\n"
" ds_read_b64_tr_b16 a[36:37], v176 offset:33792\n"
" ds_read_b64_tr_b16 a[38:39], v176 offset:34304\n"
" buffer_load_dwordx4 v7, s[20:23], s44 offen offset:3072 lds\n"
" s_add_u32 m0, s72, 4294852608\n"
" ds_read_b64_tr_b16 a[40:41], v176 offset:34816\n"
" ds_read_b64_tr_b16 a[42:43], v176 offset:35328\n"
" ds_read_b64_tr_b16 a[44:45], v176 offset:35840\n"
" ds_read_b64_tr_b16 a[46:47], v176 offset:36352\n"
" ds_read_b64_tr_b16 a[48:49], v176 offset:49152\n"
" ds_read_b64_tr_b16 a[50:51], v176 offset:49664\n"
" ds_read_b64_tr_b16 a[52:53], v176 offset:50176\n"
" ds_read_b64_tr_b16 a[54:55], v176 offset:50688\n"
" ds_read_b64_tr_b16 a[56:57], v176 offset:51200\n"
" ds_read_b64_tr_b16 a[58:59], v176 offset:51712\n"
" ds_read_b64_tr_b16 a[60:61], v176 offset:52224\n"
" ds_read_b64_tr_b16 a[62:63], v176 offset:52736\n"
".LmodA_chk:\n"
" s_waitcnt vmcnt(7)\n"
" v_xor_b32 v212, s52, v212\n"
" v_xor_b32 v213, s53, v213\n"
" v_xor_b32 v214, s54, v214\n"
" v_xor_b32 v215, s55, v215\n"
" s_waitcnt vmcnt(6)\n"
" v_xor_b32 v216, s52, v216\n"
" v_xor_b32 v217, s53, v217\n"
" v_xor_b32 v218, s54, v218\n"
" v_xor_b32 v219, s55, v219\n"
" s_waitcnt vmcnt(5)\n"
" v_xor_b32 v220, s52, v220\n"
" v_xor_b32 v221, s53, v221\n"
" v_xor_b32 v222, s54, v222\n"
" v_xor_b32 v223, s55, v223\n"
" s_waitcnt vmcnt(4)\n"
" v_xor_b32 v224, s52, v224\n"
" v_xor_b32 v225, s53, v225\n"
" v_xor_b32 v226, s54, v226\n"
" v_xor_b32 v227, s55, v227\n"
" v_or_b32_e64 v198, v212, v213\n"
" v_or3_b32 v198, v198, v214, v215\n"
" v_or3_b32 v198, v198, v216, v217\n"
" v_or3_b32 v198, v198, v218, v219\n"
" v_or3_b32 v198, v198, v220, v221\n"
" v_or3_b32 v198, v198, v222, v223\n"
" v_or3_b32 v198, v198, v224, v225\n"
" v_or3_b32 v198, v198, v226, v227\n"
" v_and_b32 v198, s74, v198\n"
" v_cmp_ne_u32 vcc, 0, v198\n"
" s_nop 0\n"
" s_cbranch_vccnz .LmodA_slow\n"
".LmodA_ok:\n"
" s_waitcnt lgkmcnt(0)\n"
" s_barrier\n"
" v_pk_add_f32 v[212:213], v[212:213], v[216:217]\n"
" v_pk_add_f32 v[220:221], v[220:221], v[224:225]\n"
" v_pk_add_f32 v[214:215], v[214:215], v[218:219]\n"
" v_pk_add_f32 v[222:223], v[222:223], v[226:227]\n"
" v_pk_add_f32 v[212:213], v[212:213], v[220:221]\n"
" v_pk_add_f32 v[214:215], v[214:215], v[222:223]\n"
" v_add_f32 v212, v187, v212\n"
" v_add_f32 v213, v187, v213\n"
" v_add_f32 v214, v187, v214\n"
" v_add_f32 v215, v187, v215\n"
" v_cvt_pk_bf16_f32 v212, v212, v212\n"
" v_cvt_pk_bf16_f32 v213, v213, v213\n"
" v_cvt_pk_bf16_f32 v214, v214, v214\n"
" v_cvt_pk_bf16_f32 v215, v215, v215\n"
" ds_write_b128 v182, v[212:215]\n"
" s_mov_b32 exec_lo, 0x00f0000f\n"
" s_mov_b32 exec_hi, 0xf0000f00\n"
" ds_read2_b32 v[144:145], v183 offset0:128 offset1:0\n"
" ds_read2_b32 v[150:151], v183 offset0:132 offset1:4\n"
" ds_read2_b32 v[152:153], v183 offset0:160 offset1:32\n"
" ds_read2_b32 v[158:159], v183 offset0:164 offset1:36\n"
" ds_read2_b32 v[160:161], v183 offset0:192 offset1:64\n"
" ds_read2_b32 v[166:167], v183 offset0:196 offset1:68\n"
" ds_read2_b32 v[168:169], v183 offset0:224 offset1:96\n"
" ds_read2_b32 v[174:175], v183 offset0:228 offset1:100\n"
" s_mov_b64 exec, -1\n"
" s_mov_b32 m0, s72\n"
" s_nop 0\n"
" buffer_load_dwordx4 v184, s[24:27], s84 offen nt lds\n"
" buffer_load_dwordx4 v203, s[24:27], s84 offen offset:1024 nt lds\n"
" s_add_u32 m0, m0, 2048\n"
" s_nop 0\n"
" buffer_load_dwordx4 v184, s[28:31], s95 offen nt lds\n"
" s_waitcnt lgkmcnt(7)\n"
" v_and_b32 v146, v236, v144\n"
" v_and_b32 v147, v237, v144\n"
" s_waitcnt lgkmcnt(6)\n"
" v_and_b32 v148, v236, v150\n"
" v_and_b32 v149, v237, v150\n"
" s_waitcnt lgkmcnt(5)\n"
" v_and_b32 v154, v236, v152\n"
" v_and_b32 v155, v237, v152\n"
" s_waitcnt lgkmcnt(4)\n"
" v_and_b32 v156, v236, v158\n"
" v_and_b32 v157, v237, v158\n"
" v_mfma_f32_16x16x32_bf16 v[120:123], v[144:147], v[16:19], 0\n"
" s_waitcnt lgkmcnt(3)\n"
" v_and_b32 v162, v236, v160\n"
" v_and_b32 v163, v237, v160\n"
" v_mfma_f32_16x16x32_bf16 v[124:127], v[148:151], v[20:23], 0\n"
" s_waitcnt lgkmcnt(2)\n"
" v_and_b32 v164, v236, v166\n"
" v_and_b32 v165, v237, v166\n"
" v_mfma_f32_16x16x32_bf16 v[128:131], v[144:147], v[42:45], 0\n"
" s_waitcnt lgkmcnt(1)\n"
" v_and_b32 v170, v236, v168\n"
" v_and_b32 v171, v237, v168\n"
" v_mfma_f32_16x16x32_bf16 v[132:135], v[148:151], v[46:49], 0\n"
" s_waitcnt lgkmcnt(0)\n"
" v_and_b32 v172, v236, v174\n"
" v_and_b32 v173, v237, v174\n"
" v_mfma_f32_16x16x32_bf16 v[136:139], v[144:147], v[68:71], 0\n"
" v_cvt_pk_bf16_f32 v216, v120, v121\n"
" v_cvt_pk_bf16_f32 v217, v122, v123\n"
" v_mfma_f32_16x16x32_bf16 v[140:143], v[148:151], v[72:75], 0\n"
" v_cvt_pk_bf16_f32 v218, v124, v125\n"
" v_cvt_pk_bf16_f32 v219, v126, v127\n"
" v_mfma_f32_16x16x32_bf16 v[120:123], v[144:147], v[94:97], 0\n"
" ds_write_b128 v206, v[216:219] offset:0\n"
" buffer_load_dwordx4 v203, s[28:31], s95 offen offset:1024 nt lds\n"
" v_cvt_pk_bf16_f32 v220, v128, v129\n"
" v_cvt_pk_bf16_f32 v221, v130, v131\n"
" v_mfma_f32_16x16x32_bf16 v[124:127], v[148:151], v[98:101], 0\n"
" v_cvt_pk_bf16_f32 v222, v132, v133\n"
" v_cvt_pk_bf16_f32 v223, v134, v135\n"
" v_mfma_f32_16x16x32_bf16 v[128:131], v[152:155], v[22:25], 0\n"
" ds_write_b128 v206, v[220:223] offset:1024\n"
" s_mov_b64 exec, s[92:93]\n"
" buffer_load_dword v254, v186, s[4:7], 0 offen\n"
" s_mov_b64 exec, -1\n"
" v_cvt_pk_bf16_f32 v216, v136, v137\n"
" v_cvt_pk_bf16_f32 v217, v138, v139\n"
" v_mfma_f32_16x16x32_bf16 v[132:135], v[156:159], v[26:29], 0\n"
" v_cvt_pk_bf16_f32 v218, v140, v141\n"
" v_cvt_pk_bf16_f32 v219, v142, v143\n"
" v_mfma_f32_16x16x32_bf16 v[136:139], v[152:155], v[48:51], 0\n"
" ds_write_b128 v206, v[216:219] offset:2048\n"
" s_mov_b64 exec, 15\n"
" buffer_load_dword v254, v185, s[32:35], 0 offen\n"
" s_mov_b64 exec, -1\n"
" v_cvt_pk_bf16_f32 v220, v120, v121\n"
" v_cvt_pk_bf16_f32 v221, v122, v123\n"
" v_mfma_f32_16x16x32_bf16 v[140:143], v[156:159], v[52:55], 0\n"
" v_cvt_pk_bf16_f32 v222, v124, v125\n"
" v_cvt_pk_bf16_f32 v223, v126, v127\n"
" v_mfma_f32_16x16x32_bf16 v[120:123], v[152:155], v[74:77], 0\n"
" ds_write_b128 v206, v[220:223] offset:3072\n"
" v_cvt_pk_bf16_f32 v216, v128, v129\n"
" v_cvt_pk_bf16_f32 v217, v130, v131\n"
" v_mfma_f32_16x16x32_bf16 v[124:127], v[156:159], v[78:81], 0\n"
" v_cvt_pk_bf16_f32 v218, v132, v133\n"
" v_cvt_pk_bf16_f32 v219, v134, v135\n"
" v_mfma_f32_16x16x32_bf16 v[128:131], v[152:155], v[100:103], 0\n"
" ds_write_b128 v206, v[216:219] offset:4096\n"
" v_cvt_pk_bf16_f32 v220, v136, v137\n"
" v_cvt_pk_bf16_f32 v221, v138, v139\n"
" v_mfma_f32_16x16x32_bf16 v[132:135], v[156:159], v[104:107], 0\n"
" v_cvt_pk_bf16_f32 v222, v140, v141\n"
" v_cvt_pk_bf16_f32 v223, v142, v143\n"
" v_mfma_f32_16x16x32_bf16 v[136:139], v[160:163], v[28:31], 0\n"
" ds_write_b128 v206, v[220:223] offset:5120\n"
" v_cvt_pk_bf16_f32 v216, v120, v121\n"
" v_cvt_pk_bf16_f32 v217, v122, v123\n"
" v_mfma_f32_16x16x32_bf16 v[140:143], v[164:167], v[32:35], 0\n"
" v_cvt_pk_bf16_f32 v218, v124, v125\n"
" v_cvt_pk_bf16_f32 v219, v126, v127\n"
" v_mfma_f32_16x16x32_bf16 v[120:123], v[160:163], v[54:57], 0\n"
" ds_write_b128 v206, v[216:219] offset:6144\n"
" v_cvt_pk_bf16_f32 v220, v128, v129\n"
" v_cvt_pk_bf16_f32 v221, v130, v131\n"
" v_mfma_f32_16x16x32_bf16 v[124:127], v[164:167], v[58:61], 0\n"
" v_cvt_pk_bf16_f32 v222, v132, v133\n"
" v_cvt_pk_bf16_f32 v223, v134, v135\n"
" v_mfma_f32_16x16x32_bf16 v[128:131], v[160:163], v[80:83], 0\n"
" ds_write_b128 v206, v[220:223] offset:7168\n"
" v_cvt_pk_bf16_f32 v216, v136, v137\n"
" v_cvt_pk_bf16_f32 v217, v138, v139\n"
" v_mfma_f32_16x16x32_bf16 v[132:135], v[164:167], v[84:87], 0\n"
" v_cvt_pk_bf16_f32 v218, v140, v141\n"
" v_cvt_pk_bf16_f32 v219, v142, v143\n"
" v_mfma_f32_16x16x32_bf16 v[136:139], v[160:163], v[106:109], 0\n"
" ds_write_b128 v207, v[216:219] offset:0\n"
" v_cvt_pk_bf16_f32 v220, v120, v121\n"
" v_cvt_pk_bf16_f32 v221, v122, v123\n"
" v_mfma_f32_16x16x32_bf16 v[140:143], v[164:167], v[110:113], 0\n"
" v_cvt_pk_bf16_f32 v222, v124, v125\n"
" v_cvt_pk_bf16_f32 v223, v126, v127\n"
" v_mfma_f32_16x16x32_bf16 v[120:123], v[168:171], v[34:37], 0\n"
" ds_write_b128 v207, v[220:223] offset:1024\n"
" v_cvt_pk_bf16_f32 v216, v128, v129\n"
" v_cvt_pk_bf16_f32 v217, v130, v131\n"
" v_mfma_f32_16x16x32_bf16 v[124:127], v[172:175], v[38:41], 0\n"
" v_cvt_pk_bf16_f32 v218, v132, v133\n"
" v_cvt_pk_bf16_f32 v219, v134, v135\n"
" v_mfma_f32_16x16x32_bf16 v[128:131], v[168:171], v[60:63], 0\n"
" ds_write_b128 v207, v[216:219] offset:2048\n"
" v_cvt_pk_bf16_f32 v220, v136, v137\n"
" v_cvt_pk_bf16_f32 v221, v138, v139\n"
" v_mfma_f32_16x16x32_bf16 v[132:135], v[172:175], v[64:67], 0\n"
" v_cvt_pk_bf16_f32 v222, v140, v141\n"
" v_cvt_pk_bf16_f32 v223, v142, v143\n"
" v_mfma_f32_16x16x32_bf16 v[136:139], v[168:171], v[86:89], 0\n"
" ds_write_b128 v207, v[220:223] offset:3072\n"
" v_cvt_pk_bf16_f32 v216, v120, v121\n"
" v_cvt_pk_bf16_f32 v217, v122, v123\n"
" v_mfma_f32_16x16x32_bf16 v[140:143], v[172:175], v[90:93], 0\n"
" v_cvt_pk_bf16_f32 v218, v124, v125\n"
" v_cvt_pk_bf16_f32 v219, v126, v127\n"
" v_mfma_f32_16x16x32_bf16 v[120:123], v[168:171], v[112:115], 0\n"
" ds_write_b128 v207, v[216:219] offset:4096\n"
" v_cvt_pk_bf16_f32 v220, v128, v129\n"
" v_cvt_pk_bf16_f32 v221, v130, v131\n"
" v_mfma_f32_16x16x32_bf16 v[124:127], v[172:175], v[116:119], 0\n"
" v_cvt_pk_bf16_f32 v222, v132, v133\n"
" v_cvt_pk_bf16_f32 v223, v134, v135\n"
" ds_write_b128 v207, v[220:223] offset:5120\n"
" v_cvt_pk_bf16_f32 v216, v136, v137\n"
" v_cvt_pk_bf16_f32 v217, v138, v139\n"
" v_cvt_pk_bf16_f32 v218, v140, v141\n"
" v_cvt_pk_bf16_f32 v219, v142, v143\n"
" ds_write_b128 v207, v[216:219] offset:6144\n"
" v_cvt_pk_bf16_f32 v220, v120, v121\n"
" v_cvt_pk_bf16_f32 v221, v122, v123\n"
" v_cvt_pk_bf16_f32 v222, v124, v125\n"
" v_cvt_pk_bf16_f32 v223, v126, v127\n"
" ds_write_b128 v207, v[220:223] offset:7168\n"
" s_waitcnt lgkmcnt(0)\n"
" s_barrier\n"
" ds_read_b128 v[16:19], v201 offset:0\n"
" ds_read_b128 v[20:23], v201 offset:1024\n"
" ds_read_b128 v[24:27], v201 offset:2048\n"
" ds_read_b128 v[28:31], v201 offset:3072\n"
" s_add_u32 m0, s72, 4294852608\n"
" s_nop 0\n"
" buffer_load_dwordx4 v6, s[20:23], s45 offen offset:0 lds\n"
" buffer_load_dwordx4 v6, s[20:23], s45 offen offset:1024 lds\n"
" buffer_load_dwordx4 v7, s[20:23], s45 offen offset:2048 lds\n"
" s_lshl_b32 s67, s57, 7\n"
" s_add_u32 s67, s67, s59\n"
" s_lshl_b32 s67, s67, 9\n"
" s_add_u32 s85, s67, 2621440\n"
" s_lshl2_add_u32 s67, s62, s3\n"
" s_and_b32 s68, s57, 1\n"
" s_lshl1_add_u32 s67, s67, s68\n"
" s_lshl3_add_u32 s67, s67, s58\n"
" s_lshl_b32 s68, s67, 11\n"
" s_add_u32 s92, s68, 4718592\n"
" s_lshl_b32 s68, s67, 7\n"
" s_add_u32 s93, s68, 294912\n"
" s_and_b32 s68, s57, 1\n"
" s_mul_i32 s68, s68, 4\n"
" s_waitcnt lgkmcnt(3)\n"
" v_mfma_f32_16x16x32_bf16 v[64:67], a[0:3], v[16:19], 0\n"
" s_lshl_b32 s61, 252641280, s68\n"
" ds_read_b128 v[32:35], v201 offset:4096\n"
" s_lshl_b32 s68, s59, 5\n"
" v_mfma_f32_16x16x32_bf16 v[80:83], v[16:19], a[4:7], 0\n"
" s_add_u32 s68, s68, s57\n"
" ds_read_b128 v[36:39], v201 offset:5120\n"
" s_lshl_b32 s67, s68, 7\n"
" s_waitcnt lgkmcnt(4)\n"
" v_mfma_f32_16x16x32_bf16 v[68:71], a[0:3], v[20:23], 0\n"
" s_add_u32 s67, s67, 163840\n"
" ds_read_b128 v[40:43], v201 offset:6144\n"
" s_lshl_b32 s69, s68, 11\n"
" v_mfma_f32_16x16x32_bf16 v[84:87], v[20:23], a[4:7], 0\n"
" s_add_u32 s69, s69, 524288\n"
" ds_read_b128 v[44:47], v201 offset:7168\n"
" s_lshl_b32 s70, s68, 6\n"
" s_waitcnt lgkmcnt(5)\n"
" v_mfma_f32_16x16x32_bf16 v[72:75], a[0:3], v[24:27], 0\n"
" s_add_u32 s70, s70, 65536\n"
" buffer_load_dwordx4 v7, s[20:23], s45 offen offset:3072 lds\n"
" s_add_u32 m0, s72, 4294868992\n"
" v_mfma_f32_16x16x32_bf16 v[88:91], v[24:27], a[4:7], 0\n"
" s_lshl_b32 s65, s59, 10\n"
" s_add_u32 s65, s65, s57\n"
" s_lshl_b32 s65, s65, 6\n"
" s_waitcnt lgkmcnt(4)\n"
" v_mfma_f32_16x16x32_bf16 v[76:79], a[0:3], v[28:31], 0\n"
" s_add_u32 s65, s65, 524288\n"
" s_add_u32 s68, s65, 32768\n"
" v_xor_b32 v12, v243, v12\n"
" v_mfma_f32_16x16x32_bf16 v[92:95], v[28:31], a[4:7], 0\n"
" buffer_load_dwordx4 v6, s[20:23], s46 offen offset:0 lds\n"
" v_xor_b32 v13, v243, v13\n"
" s_waitcnt lgkmcnt(0)\n"
" v_mfma_f32_16x16x32_bf16 v[64:67], a[8:11], v[32:35], v[64:67]\n"
" ds_read_b128 v[16:19], v201 offset:8192\n"
" v_xor_b32 v14, v243, v14\n"
" v_mfma_f32_16x16x32_bf16 v[80:83], v[32:35], a[12:15], v[80:83]\n"
" ds_read_b128 v[20:23], v201 offset:9216\n"
" v_xor_b32_e64 v15, v243, v15\n"
" v_mfma_f32_16x16x32_bf16 v[68:71], a[8:11], v[36:39], v[68:71]\n"
" ds_read_b128 v[24:27], v201 offset:10240\n"
" v_lshl_add_u32 v198, v3, 2, s100\n"
" v_mfma_f32_16x16x32_bf16 v[84:87], v[36:39], a[12:15], v[84:87]\n"
" ds_read_b128 v[28:31], v201 offset:11264\n"
" v_mfma_f32_16x16x32_bf16 v[72:75], a[8:11], v[40:43], v[72:75]\n"
" buffer_load_dwordx4 v6, s[20:23], s46 offen offset:1024 lds\n"
" v_mfma_f32_16x16x32_bf16 v[88:91], v[40:43], a[12:15], v[88:91]\n"
" s_waitcnt vmcnt(8)\n"
" ds_write_b32 v198, v208 offset:0\n"
" v_mfma_f32_16x16x32_bf16 v[76:79], a[8:11], v[44:47], v[76:79]\n"
" ds_read_b64_tr_b16 v[96:97], v248 offset:0\n"
" v_mfma_f32_16x16x32_bf16 v[92:95], v[44:47], a[12:15], v[92:95]\n"
" buffer_load_dwordx4 v7, s[20:23], s46 offen offset:2048 lds\n"
" s_waitcnt lgkmcnt(2)\n"
" v_mfma_f32_16x16x32_bf16 v[64:67], a[16:19], v[16:19], v[64:67]\n"
" ds_read_b128 v[32:35], v201 offset:12288\n"
" v_mfma_f32_16x16x32_bf16 v[80:83], v[16:19], a[20:23], v[80:83]\n"
" ds_read_b128 v[36:39], v201 offset:13312\n"
" v_mfma_f32_16x16x32_bf16 v[68:71], a[16:19], v[20:23], v[68:71]\n"
" ds_read_b128 v[40:43], v201 offset:14336\n"
" v_mfma_f32_16x16x32_bf16 v[84:87], v[20:23], a[20:23], v[84:87]\n"
" ds_read_b128 v[44:47], v201 offset:15360\n"
" v_mfma_f32_16x16x32_bf16 v[72:75], a[16:19], v[24:27], v[72:75]\n"
" buffer_load_dwordx4 v7, s[20:23], s46 offen offset:3072 lds\n"
" s_add_u32 m0, s72, 4294885376\n"
" v_mfma_f32_16x16x32_bf16 v[88:91], v[24:27], a[20:23], v[88:91]\n"
" ds_read_b64_tr_b16 v[98:99], v248 offset:1024\n"
" v_mfma_f32_16x16x32_bf16 v[76:79], a[16:19], v[28:31], v[76:79]\n"
" ds_read_b64_tr_b16 v[100:101], v249 offset:0\n"
" v_mfma_f32_16x16x32_bf16 v[92:95], v[28:31], a[20:23], v[92:95]\n"
" buffer_load_dwordx4 v6, s[20:23], s47 offen offset:0 lds\n"
" s_waitcnt lgkmcnt(2)\n"
" v_mfma_f32_16x16x32_bf16 v[64:67], a[24:27], v[32:35], v[64:67]\n"
" ds_read_b128 v[16:19], v201 offset:16384\n"
" v_mfma_f32_16x16x32_bf16 v[80:83], v[32:35], a[28:31], v[80:83]\n"
" ds_read_b128 v[20:23], v201 offset:17408\n"
" v_mfma_f32_16x16x32_bf16 v[68:71], a[24:27], v[36:39], v[68:71]\n"
" ds_read_b128 v[24:27], v201 offset:18432\n"
" v_mfma_f32_16x16x32_bf16 v[84:87], v[36:39], a[28:31], v[84:87]\n"
" ds_read_b128 v[28:31], v201 offset:19456\n"
" v_mfma_f32_16x16x32_bf16 v[72:75], a[24:27], v[40:43], v[72:75]\n"
" buffer_load_dwordx4 v6, s[20:23], s47 offen offset:1024 lds\n"
" v_mfma_f32_16x16x32_bf16 v[88:91], v[40:43], a[28:31], v[88:91]\n"
" ds_read_b64_tr_b16 v[102:103], v249 offset:1024\n"
" v_mfma_f32_16x16x32_bf16 v[76:79], a[24:27], v[44:47], v[76:79]\n"
" ds_read_b64_tr_b16 v[104:105], v250 offset:0\n"
" v_mfma_f32_16x16x32_bf16 v[92:95], v[44:47], a[28:31], v[92:95]\n"
" buffer_load_dwordx4 v7, s[20:23], s47 offen offset:2048 lds\n"
" s_waitcnt lgkmcnt(2)\n"
" v_mfma_f32_16x16x32_bf16 v[64:67], a[32:35], v[16:19], v[64:67]\n"
" ds_read_b128 v[32:35], v201 offset:20480\n"
" v_mfma_f32_16x16x32_bf16 v[80:83], v[16:19], a[36:39], v[80:83]\n"
" ds_read_b128 v[36:39], v201 offset:21504\n"
" v_mfma_f32_16x16x32_bf16 v[68:71], a[32:35], v[20:23], v[68:71]\n"
" ds_read_b128 v[40:43], v201 offset:22528\n"
" v_mfma_f32_16x16x32_bf16 v[84:87], v[20:23], a[36:39], v[84:87]\n"
" ds_read_b128 v[44:47], v201 offset:23552\n"
" v_mfma_f32_16x16x32_bf16 v[72:75], a[32:35], v[24:27], v[72:75]\n"
" buffer_load_dwordx4 v7, s[20:23], s47 offen offset:3072 lds\n"
" v_mfma_f32_16x16x32_bf16 v[88:91], v[24:27], a[36:39], v[88:91]\n"
" ds_read_b64_tr_b16 v[106:107], v250 offset:1024\n"
" v_mfma_f32_16x16x32_bf16 v[76:79], a[32:35], v[28:31], v[76:79]\n"
" ds_read_b64_tr_b16 v[108:109], v251 offset:0\n"
" v_mfma_f32_16x16x32_bf16 v[92:95], v[28:31], a[36:39], v[92:95]\n"
" s_mov_b32 vcc_lo, s97\n"
" ds_read_b64_tr_b16 v[110:111], v251 offset:1024\n"
" s_waitcnt lgkmcnt(3)\n"
" v_mfma_f32_16x16x32_bf16 v[64:67], a[40:43], v[32:35], v[64:67]\n"
" s_mov_b32 vcc_hi, s97\n"
" ds_read_b128 v[16:19], v201 offset:24576\n"
" v_cndmask_b32_dpp v212, v100, v96, vcc quad_perm:[2,3,0,1] row_mask:0xf bank_mask:0xf\n"
" v_mfma_f32_16x16x32_bf16 v[80:83], v[32:35], a[44:47], v[80:83]\n"
" ds_read_b128 v[20:23], v201 offset:25600\n"
" v_cndmask_b32_dpp v213, v101, v97, vcc quad_perm:[2,3,0,1] row_mask:0xf bank_mask:0xf\n"
" v_mfma_f32_16x16x32_bf16 v[68:71], a[40:43], v[36:39], v[68:71]\n"
" ds_read_b128 v[24:27], v201 offset:26624\n"
" v_cndmask_b32_dpp v214, v102, v98, vcc quad_perm:[2,3,0,1] row_mask:0xf bank_mask:0xf\n"
" v_mfma_f32_16x16x32_bf16 v[84:87], v[36:39], a[44:47], v[84:87]\n"
" ds_read_b128 v[28:31], v201 offset:27648\n"
" v_cndmask_b32_dpp v215, v103, v99, vcc quad_perm:[2,3,0,1] row_mask:0xf bank_mask:0xf\n"
" v_mfma_f32_16x16x32_bf16 v[72:75], a[40:43], v[40:43], v[72:75]\n"
" s_waitcnt lgkmcnt(5)\n"
" v_cndmask_b32_dpp v220, v108, v104, vcc quad_perm:[2,3,0,1] row_mask:0xf bank_mask:0xf\n"
" v_cndmask_b32_dpp v221, v109, v105, vcc quad_perm:[2,3,0,1] row_mask:0xf bank_mask:0xf\n"
" v_mfma_f32_16x16x32_bf16 v[88:91], v[40:43], a[44:47], v[88:91]\n"
" s_waitcnt lgkmcnt(4)\n"
" v_cndmask_b32_dpp v222, v110, v106, vcc quad_perm:[2,3,0,1] row_mask:0xf bank_mask:0xf\n"
" v_cndmask_b32_dpp v223, v111, v107, vcc quad_perm:[2,3,0,1] row_mask:0xf bank_mask:0xf\n"
" s_mov_b32 vcc_lo, s83\n"
" v_mfma_f32_16x16x32_bf16 v[76:79], a[40:43], v[44:47], v[76:79]\n"
" s_mov_b32 vcc_hi, s83\n"
" v_cndmask_b32_dpp v216, v96, v100, vcc quad_perm:[2,3,0,1] row_mask:0xf bank_mask:0xf\n"
" v_cndmask_b32_dpp v217, v97, v101, vcc quad_perm:[2,3,0,1] row_mask:0xf bank_mask:0xf\n"
" v_mfma_f32_16x16x32_bf16 v[92:95], v[44:47], a[44:47], v[92:95]\n"
" v_cndmask_b32_dpp v218, v98, v102, vcc quad_perm:[2,3,0,1] row_mask:0xf bank_mask:0xf\n"
" v_cndmask_b32_dpp v219, v99, v103, vcc quad_perm:[2,3,0,1] row_mask:0xf bank_mask:0xf\n"
" s_waitcnt lgkmcnt(0)\n"
" v_cndmask_b32_dpp v224, v104, v108, vcc quad_perm:[2,3,0,1] row_mask:0xf bank_mask:0xf\n"
" v_cndmask_b32_dpp v225, v105, v109, vcc quad_perm:[2,3,0,1] row_mask:0xf bank_mask:0xf\n"
" v_cndmask_b32_dpp v226, v106, v110, vcc quad_perm:[2,3,0,1] row_mask:0xf bank_mask:0xf\n"
" v_cndmask_b32_dpp v227, v107, v111, vcc quad_perm:[2,3,0,1] row_mask:0xf bank_mask:0xf\n"
" s_waitcnt vmcnt(0)\n"
" s_barrier\n"
" v_mfma_f32_16x16x32_bf16 v[64:67], a[48:51], v[16:19], v[64:67]\n"
" buffer_store_dwordx4 v[212:215], v1, s[16:19], s50 offen sc1\n"
" ds_read_b128 v[32:35], v201 offset:28672\n"
" s_add_u32 s50, s50, s64\n"
" v_mfma_f32_16x16x32_bf16 v[80:83], v[16:19], a[52:55], v[80:83]\n"
" ds_read_b128 v[36:39], v201 offset:29696\n"
" v_mfma_f32_16x16x32_bf16 v[68:71], a[48:51], v[20:23], v[68:71]\n"
" ds_read_b128 v[40:43], v201 offset:30720\n"
" v_mfma_f32_16x16x32_bf16 v[84:87], v[20:23], a[52:55], v[84:87]\n"
" ds_read_b128 v[44:47], v201 offset:31744\n"
" v_mfma_f32_16x16x32_bf16 v[72:75], a[48:51], v[24:27], v[72:75]\n"
" ds_read_b64_tr_b16 a[64:65], v205 offset:0\n"
" v_mfma_f32_16x16x32_bf16 v[88:91], v[24:27], a[52:55], v[88:91]\n"
" ds_read_b64_tr_b16 a[66:67], v205 offset:512\n"
" ds_read_b64_tr_b16 a[68:69], v205 offset:1024\n"
" v_mfma_f32_16x16x32_bf16 v[76:79], a[48:51], v[28:31], v[76:79]\n"
" ds_read_b64_tr_b16 a[70:71], v205 offset:1536\n"
" ds_read_b64_tr_b16 a[72:73], v205 offset:2048\n"
" buffer_store_dwordx4 v[216:219], v1, s[16:19], s50 offen sc1\n"
" s_add_u32 s50, s50, s64\n"
" v_mfma_f32_16x16x32_bf16 v[92:95], v[28:31], a[52:55], v[92:95]\n"
" ds_read_b64_tr_b16 a[74:75], v205 offset:2560\n"
" s_waitcnt lgkmcnt(6)\n"
" v_mfma_f32_16x16x32_bf16 v[64:67], a[56:59], v[32:35], v[64:67]\n"
" ds_read_b128 v[16:19], v201 offset:32768\n"
" v_mfma_f32_16x16x32_bf16 v[80:83], v[32:35], a[60:63], v[80:83]\n"
" ds_read_b128 v[20:23], v201 offset:33792\n"
" v_mfma_f32_16x16x32_bf16 v[68:71], a[56:59], v[36:39], v[68:71]\n"
" ds_read_b128 v[24:27], v201 offset:34816\n"
" v_mfma_f32_16x16x32_bf16 v[84:87], v[36:39], a[60:63], v[84:87]\n"
" ds_read_b128 v[28:31], v201 offset:35840\n"
" v_mfma_f32_16x16x32_bf16 v[72:75], a[56:59], v[40:43], v[72:75]\n"
" ds_read_b64_tr_b16 a[76:77], v205 offset:3072\n"
" buffer_store_dwordx4 v[220:223], v1, s[16:19], s50 offen sc1\n"
" s_add_u32 s50, s50, s64\n"
" v_mfma_f32_16x16x32_bf16 v[88:91], v[40:43], a[60:63], v[88:91]\n"
" ds_read_b64_tr_b16 a[78:79], v205 offset:3584\n"
" ds_read_b64_tr_b16 a[80:81], v205 offset:16384\n"
" v_mfma_f32_16x16x32_bf16 v[76:79], a[56:59], v[44:47], v[76:79]\n"
" ds_read_b64_tr_b16 a[82:83], v205 offset:16896\n"
" ds_read_b64_tr_b16 a[84:85], v205 offset:17408\n"
" v_mfma_f32_16x16x32_bf16 v[92:95], v[44:47], a[60:63], v[92:95]\n"
" ds_read_b64_tr_b16 a[86:87], v205 offset:17920\n"
" s_waitcnt lgkmcnt(6)\n"
" v_mfma_f32_16x16x32_bf16 v[64:67], a[64:67], v[16:19], v[64:67]\n"
" ds_read_b128 v[32:35], v201 offset:36864\n"
" v_mfma_f32_16x16x32_bf16 v[80:83], v[16:19], a[68:71], v[80:83]\n"
" ds_read_b128 v[36:39], v201 offset:37888\n"
" v_mfma_f32_16x16x32_bf16 v[68:71], a[64:67], v[20:23], v[68:71]\n"
" buffer_store_dwordx4 v[224:227], v1, s[16:19], s50 offen sc1\n"
" ds_read_b128 v[40:43], v201 offset:38912\n"
" v_mfma_f32_16x16x32_bf16 v[84:87], v[20:23], a[68:71], v[84:87]\n"
" ds_read_b128 v[44:47], v201 offset:39936\n"
" v_mfma_f32_16x16x32_bf16 v[72:75], a[64:67], v[24:27], v[72:75]\n"
" ds_read_b64_tr_b16 a[88:89], v205 offset:18432\n"
" v_mfma_f32_16x16x32_bf16 v[88:91], v[24:27], a[68:71], v[88:91]\n"
" ds_read_b64_tr_b16 a[90:91], v205 offset:18944\n"
" ds_read_b64_tr_b16 a[92:93], v205 offset:19456\n"
" v_mfma_f32_16x16x32_bf16 v[76:79], a[64:67], v[28:31], v[76:79]\n"
" ds_read_b64_tr_b16 a[94:95], v205 offset:19968\n"
" ds_read_b64_tr_b16 a[96:97], v205 offset:32768\n"
" v_mfma_f32_16x16x32_bf16 v[92:95], v[28:31], a[68:71], v[92:95]\n"
" ds_read_b64_tr_b16 a[98:99], v205 offset:33280\n"
" s_waitcnt lgkmcnt(6)\n"
" v_mfma_f32_16x16x32_bf16 v[64:67], a[72:75], v[32:35], v[64:67]\n"
" ds_read_b128 v[16:19], v201 offset:40960\n"
" v_mfma_f32_16x16x32_bf16 v[80:83], v[32:35], a[76:79], v[80:83]\n"
" ds_read_b128 v[20:23], v201 offset:41984\n"
" v_mfma_f32_16x16x32_bf16 v[68:71], a[72:75], v[36:39], v[68:71]\n"
" ds_read_b128 v[24:27], v201 offset:43008\n"
" v_mfma_f32_16x16x32_bf16 v[84:87], v[36:39], a[76:79], v[84:87]\n"
" ds_read_b128 v[28:31], v201 offset:44032\n"
" v_mfma_f32_16x16x32_bf16 v[72:75], a[72:75], v[40:43], v[72:75]\n"
" ds_read_b64_tr_b16 a[100:101], v205 offset:33792\n"
" v_mfma_f32_16x16x32_bf16 v[88:91], v[40:43], a[76:79], v[88:91]\n"
" ds_read_b64_tr_b16 a[102:103], v205 offset:34304\n"
" ds_read_b64_tr_b16 a[104:105], v205 offset:34816\n"
" v_mfma_f32_16x16x32_bf16 v[76:79], a[72:75], v[44:47], v[76:79]\n"
" ds_read_b64_tr_b16 a[106:107], v205 offset:35328\n"
" ds_read_b64_tr_b16 a[108:109], v205 offset:35840\n"
" v_mfma_f32_16x16x32_bf16 v[92:95], v[44:47], a[76:79], v[92:95]\n"
" ds_read_b64_tr_b16 a[110:111], v205 offset:36352\n"
" s_waitcnt lgkmcnt(6)\n"
" v_mfma_f32_16x16x32_bf16 v[64:67], a[80:83], v[16:19], v[64:67]\n"
" ds_read_b128 v[32:35], v201 offset:45056\n"
" v_mfma_f32_16x16x32_bf16 v[80:83], v[16:19], a[84:87], v[80:83]\n"
" ds_read_b128 v[36:39], v201 offset:46080\n"
" v_mfma_f32_16x16x32_bf16 v[68:71], a[80:83], v[20:23], v[68:71]\n"
" ds_read_b128 v[40:43], v201 offset:47104\n"
" v_mfma_f32_16x16x32_bf16 v[84:87], v[20:23], a[84:87], v[84:87]\n"
" ds_read_b128 v[44:47], v201 offset:48128\n"
" v_mfma_f32_16x16x32_bf16 v[72:75], a[80:83], v[24:27], v[72:75]\n"
" ds_read_b64_tr_b16 a[112:113], v205 offset:49152\n"
" v_mfma_f32_16x16x32_bf16 v[88:91], v[24:27], a[84:87], v[88:91]\n"
" ds_read_b64_tr_b16 a[114:115], v205 offset:49664\n"
" ds_read_b64_tr_b16 a[116:117], v205 offset:50176\n"
" v_mfma_f32_16x16x32_bf16 v[76:79], a[80:83], v[28:31], v[76:79]\n"
" ds_read_b64_tr_b16 a[118:119], v205 offset:50688\n"
" ds_read_b64_tr_b16 a[120:121], v205 offset:51200\n"
" v_mfma_f32_16x16x32_bf16 v[92:95], v[28:31], a[84:87], v[92:95]\n"
" ds_read_b64_tr_b16 a[122:123], v205 offset:51712\n"
" s_waitcnt lgkmcnt(6)\n"
" v_mfma_f32_16x16x32_bf16 v[64:67], a[88:91], v[32:35], v[64:67]\n"
" ds_read_b128 v[16:19], v201 offset:49152\n"
" v_mfma_f32_16x16x32_bf16 v[80:83], v[32:35], a[92:95], v[80:83]\n"
" ds_read_b128 v[20:23], v201 offset:50176\n"
" v_mfma_f32_16x16x32_bf16 v[68:71], a[88:91], v[36:39], v[68:71]\n"
" ds_read_b128 v[24:27], v201 offset:51200\n"
" v_mfma_f32_16x16x32_bf16 v[84:87], v[36:39], a[92:95], v[84:87]\n"
" ds_read_b128 v[28:31], v201 offset:52224\n"
" v_mfma_f32_16x16x32_bf16 v[72:75], a[88:91], v[40:43], v[72:75]\n"
" ds_read_b64_tr_b16 a[124:125], v205 offset:52224\n"
" v_mfma_f32_16x16x32_bf16 v[88:91], v[40:43], a[92:95], v[88:91]\n"
" ds_read_b64_tr_b16 a[126:127], v205 offset:52736\n"
" v_mfma_f32_16x16x32_bf16 v[76:79], a[88:91], v[44:47], v[76:79]\n"
" ds_write_b32 v198, v209 offset:64\n"
" v_mfma_f32_16x16x32_bf16 v[92:95], v[44:47], a[92:95], v[92:95]\n"
" ds_write_b32 v198, v210 offset:128\n"
" s_waitcnt lgkmcnt(4)\n"
" s_waitcnt lgkmcnt(2)\n"
" s_barrier\n"
" s_lshl_b32 m0, s3, 11\n"
" v_mfma_f32_16x16x32_bf16 v[64:67], a[96:99], v[16:19], v[64:67]\n"
" ds_read_b128 v[32:35], v201 offset:53248\n"
" v_mfma_f32_16x16x32_bf16 v[80:83], v[16:19], a[100:103], v[80:83]\n"
" ds_read_b128 v[36:39], v201 offset:54272\n"
" v_mfma_f32_16x16x32_bf16 v[68:71], a[96:99], v[20:23], v[68:71]\n"
" ds_read_b128 v[40:43], v201 offset:55296\n"
" v_mfma_f32_16x16x32_bf16 v[84:87], v[20:23], a[100:103], v[84:87]\n"
" ds_read_b128 v[44:47], v201 offset:56320\n"
" v_mfma_f32_16x16x32_bf16 v[72:75], a[96:99], v[24:27], v[72:75]\n"
" buffer_load_dwordx4 v177, s[20:23], s40 offen lds\n"
" v_mfma_f32_16x16x32_bf16 v[88:91], v[24:27], a[100:103], v[88:91]\n"
" ds_write_b32 v198, v211 offset:192\n"
" v_mfma_f32_16x16x32_bf16 v[76:79], a[96:99], v[28:31], v[76:79]\n"
" s_mov_b32 vcc_lo, 0xffff0000\n"
" v_lshl_add_u32 v198, v2, 4, s100\n"
" s_mov_b32 vcc_hi, 0xffff0000\n"
" v_mfma_f32_16x16x32_bf16 v[92:95], v[28:31], a[100:103], v[92:95]\n"
" ds_read_b128 v[48:51], v198 offset:0\n"
" s_waitcnt lgkmcnt(2)\n"
" v_mfma_f32_16x16x32_bf16 v[64:67], a[104:107], v[32:35], v[64:67]\n"
" ds_read_b128 v[16:19], v201 offset:57344\n"
" v_mfma_f32_16x16x32_bf16 v[80:83], v[32:35], a[108:111], v[80:83]\n"
" ds_read_b128 v[20:23], v201 offset:58368\n"
" v_mfma_f32_16x16x32_bf16 v[68:71], a[104:107], v[36:39], v[68:71]\n"
" ds_read_b128 v[24:27], v201 offset:59392\n"
" v_mfma_f32_16x16x32_bf16 v[84:87], v[36:39], a[108:111], v[84:87]\n"
" ds_read_b128 v[28:31], v201 offset:60416\n"
" v_mfma_f32_16x16x32_bf16 v[72:75], a[104:107], v[40:43], v[72:75]\n"
" buffer_load_dwordx4 v7, s[20:23], s40 offen offset:1024 lds\n"
" ds_read_b128 v[128:131], v201 offset:61440\n"
" s_add_u32 m0, m0, 8192\n"
" v_mfma_f32_16x16x32_bf16 v[88:91], v[40:43], a[108:111], v[88:91]\n"
" ds_read_b128 v[132:135], v201 offset:62464\n"
" v_mfma_f32_16x16x32_bf16 v[76:79], a[104:107], v[44:47], v[76:79]\n"
" ds_read_b128 v[136:139], v201 offset:63488\n"
" v_mfma_f32_16x16x32_bf16 v[92:95], v[44:47], a[108:111], v[92:95]\n"
" ds_read_b128 v[140:143], v201 offset:64512\n"
" s_waitcnt lgkmcnt(4)\n"
" v_mfma_f32_16x16x32_bf16 v[64:67], a[112:115], v[16:19], v[64:67]\n"
" ds_read_b128 v[52:55], v198 offset:64\n"
" v_mfma_f32_16x16x32_bf16 v[68:71], a[112:115], v[20:23], v[68:71]\n"
" ds_read_b128 v[56:59], v198 offset:128\n"
" v_mfma_f32_16x16x32_bf16 v[72:75], a[112:115], v[24:27], v[72:75]\n"
" ds_read_b128 v[60:63], v198 offset:192\n"
" v_cndmask_b32 v202, v208, v209, vcc\n"
" v_mfma_f32_16x16x32_bf16 v[76:79], a[112:115], v[28:31], v[76:79]\n"
" v_mul_f32 v202, s76, v202\n"
" v_cndmask_b32 v203, v210, v211, vcc\n"
" s_waitcnt lgkmcnt(6)\n"
" v_mfma_f32_16x16x32_bf16 v[64:67], a[120:123], v[128:131], v[64:67]\n"
" buffer_load_dwordx4 v177, s[20:23], s41 offen lds\n"
" v_mul_f32 v203, s76, v203\n"
" s_waitcnt lgkmcnt(5)\n"
" v_mfma_f32_16x16x32_bf16 v[68:71], a[120:123], v[132:135], v[68:71]\n"
" v_lshlrev_b32 v198, 3, v2\n"
" v_sub_u32 v198, v3, v198\n"
" s_waitcnt lgkmcnt(4)\n"
" v_mfma_f32_16x16x32_bf16 v[72:75], a[120:123], v[136:139], v[72:75]\n"
" v_and_b32 v199, 1, v3\n"
" v_lshlrev_b32 v199, 4, v199\n"
" s_waitcnt lgkmcnt(3)\n"
" v_mfma_f32_16x16x32_bf16 v[76:79], a[120:123], v[140:143], v[76:79]\n"
" v_mov_b32 v200, 0x3f80\n"
" buffer_load_dwordx4 v7, s[20:23], s41 offen offset:1024 lds\n"
" s_add_u32 m0, m0, 8192\n"
" v_lshlrev_b32 v199, v199, v200\n"
" v_ashrrev_i32 v198, 1, v198\n"
" v_cmp_eq_i32 vcc, 0, v198\n"
" s_nop 1\n"
" v_cndmask_b32 v144, 0, v199, vcc\n"
" v_cmp_eq_i32 vcc, 1, v198\n"
" s_nop 1\n"
" v_cndmask_b32 v145, 0, v199, vcc\n"
" v_cmp_eq_i32 vcc, 2, v198\n"
" s_nop 1\n"
" v_cndmask_b32 v146, 0, v199, vcc\n"
" v_cmp_eq_i32 vcc, 3, v198\n"
" s_nop 1\n"
" v_cndmask_b32 v147, 0, v199, vcc\n"
" v_cmp_eq_i32 vcc, -8, v198\n"
" s_nop 1\n"
" v_cndmask_b32 v148, 0, v199, vcc\n"
" v_cmp_eq_i32 vcc, -7, v198\n"
" s_nop 1\n"
" v_cndmask_b32 v149, 0, v199, vcc\n"
" v_cmp_eq_i32 vcc, -6, v198\n"
" s_nop 1\n"
" v_cndmask_b32 v150, 0, v199, vcc\n"
" v_cmp_eq_i32 vcc, -5, v198\n"
" s_nop 1\n"
" v_cndmask_b32 v151, 0, v199, vcc\n"
" s_lshl_b32 s71, s3, 6\n"
" s_add_u32 s71, s71, s58\n"
" v_lshl_add_u32 v8, v5, 10, v4\n"
" v_lshl_add_u32 v8, s71, 7, v8\n"
" v_add_u32 v9, 31744, v8\n"
" v_add_u32 v10, 63488, v8\n"
" v_add_u32 v11, 95232, v8\n"
" v_mul_u32_u24 v193, 12288, v2\n"
" v_bfe_u32 v198, v0, 2, 1\n"
" v_mul_u32_u24 v198, 6144, v198\n"
" v_and_b32 v199, 0x30, v1\n"
" v_add3_u32 v193, v193, v198, v199\n"
" v_lshl_add_u32 v194, v2, 8, v1\n"
" v_and_b32 v198, 1, v2\n"
" v_lshl_add_u32 v195, v198, 6, s100\n"
" v_lshrrev_b32 v198, 1, v2\n"
" v_lshl_add_u32 v195, v198, 4, v195\n"
" s_lshr_b32 s71, s2, 6\n"
" s_lshl_b32 s71, s71, 14\n"
" v_and_b32 v196, 0x70, v1\n"
" v_lshrrev_b32 v198, 3, v0\n"
" v_lshl_add_u32 v196, v198, 10, v196\n"
" v_add_u32 v197, 7168, v196\n"
" v_bfe_u32 v198, v0, 2, 1\n"
" v_bfe_u32 v199, v0, 4, 1\n"
" v_lshl_add_u32 v198, v199, 1, v198\n"
" v_bfe_u32 v199, v0, 3, 1\n"
" v_lshl_add_u32 v198, v199, 2, v198\n"
" v_lshrrev_b32 v199, 5, v0\n"
" v_lshl_add_u32 v198, v199, 4, v198\n"
" v_and_b32 v192, 3, v0\n"
" v_lshlrev_b32 v192, 3, v192\n"
" v_lshl_add_u32 v192, v198, 7, v192\n"
" s_add_u32 s90, s3, 40\n"
" s_lshl_b32 s90, s90, 8\n"
" s_add_u32 s90, s90, s58\n"
" s_lshl_b32 s90, s90, 7\n"
" v_add_u32 v192, s72, v192\n"
" s_add_u32 s91, s90, 16384\n"
" buffer_load_dwordx4 v177, s[20:23], s42 offen lds\n"
" v_mfma_f32_16x16x32_bf16 v[80:83], v[16:19], a[116:119], v[80:83]\n"
" v_cvt_pk_bf16_f32 v96, v64, v65\n"
" v_cvt_pk_bf16_f32 v97, v66, v67\n"
" v_mfma_f32_16x16x32_bf16 v[84:87], v[20:23], a[116:119], v[84:87]\n"
" v_cvt_pk_bf16_f32 v98, v68, v69\n"
" v_cvt_pk_bf16_f32 v99, v70, v71\n"
" v_mfma_f32_16x16x32_bf16 v[88:91], v[24:27], a[116:119], v[88:91]\n"
" v_cvt_pk_bf16_f32 v100, v72, v73\n"
" v_cvt_pk_bf16_f32 v101, v74, v75\n"
" v_mfma_f32_16x16x32_bf16 v[92:95], v[28:31], a[116:119], v[92:95]\n"
" buffer_load_dwordx4 v7, s[20:23], s42 offen offset:1024 lds\n"
" v_cvt_pk_bf16_f32 v102, v76, v77\n"
" v_cvt_pk_bf16_f32 v103, v78, v79\n"
" s_add_u32 m0, m0, 8192\n"
" v_mfma_f32_16x16x32_bf16 v[80:83], v[128:131], a[124:127], v[80:83]\n"
" v_permlane16_swap_b32 v96, v98\n"
" v_mfma_f32_16x16x32_bf16 v[84:87], v[132:135], a[124:127], v[84:87]\n"
" v_permlane16_swap_b32 v97, v99\n"
" v_mfma_f32_16x16x32_bf16 v[88:91], v[136:139], a[124:127], v[88:91]\n"
" v_permlane16_swap_b32 v100, v102\n"
" v_mfma_f32_16x16x32_bf16 v[92:95], v[140:143], a[124:127], v[92:95]\n"
" buffer_load_dwordx4 v177, s[20:23], s43 offen lds\n"
" v_permlane16_swap_b32 v101, v103\n"
" v_mfma_f32_32x32x16_bf16 v[112:127], v[12:15], v[96:99], 0\n"
" s_nop 0\n"
" v_mfma_f32_32x32x16_bf16 v[128:143], v[12:15], v[100:103], 0\n"
" v_mul_f32 v80, v48, v80\n"
" v_mul_f32 v81, v49, v81\n"
" v_mul_f32 v82, v50, v82\n"
" v_mul_f32 v83, v51, v83\n"
" s_waitcnt lgkmcnt(2)\n"
" v_mul_f32 v84, v52, v84\n"
" v_mul_f32 v85, v53, v85\n"
" v_mul_f32 v86, v54, v86\n"
" v_mul_f32 v87, v55, v87\n"
" s_waitcnt lgkmcnt(1)\n"
" v_mul_f32 v88, v56, v88\n"
" v_mul_f32 v89, v57, v89\n"
" v_mul_f32 v90, v58, v90\n"
" v_mul_f32 v91, v59, v91\n"
" s_waitcnt lgkmcnt(0)\n"
" v_mul_f32 v92, v60, v92\n"
" buffer_load_dwordx4 v7, s[20:23], s43 offen offset:1024 lds\n"
" s_add_u32 m0, m0, 8192\n"
" v_mul_f32 v93, v61, v93\n"
" v_mul_f32 v94, v62, v94\n"
" v_mul_f32 v95, v63, v95\n"
" v_cvt_pk_bf16_f32 v168, v80, v81\n"
" v_cvt_pk_bf16_f32 v169, v82, v83\n"
" v_cvt_pk_bf16_f32 v170, v84, v85\n"
" v_cvt_pk_bf16_f32 v171, v86, v87\n"
" v_cvt_pk_bf16_f32 v172, v88, v89\n"
" v_cvt_pk_bf16_f32 v173, v90, v91\n"
" v_cvt_pk_bf16_f32 v174, v92, v93\n"
" v_cvt_pk_bf16_f32 v175, v94, v95\n"
" ; fm_gap\n"
" v_max3_f32 v152, v112, v113, v114\n"
" v_max3_f32 v153, v115, v116, v117\n"
" v_max3_f32 v153, v153, v118, v119\n"
" v_max3_f32 v152, v152, v120, v121\n"
" v_max3_f32 v153, v153, v122, v123\n"
" v_max3_f32 v152, v152, v124, v125\n"
" v_max3_f32 v152, v152, v126, v127\n"
" buffer_load_dwordx4 v177, s[20:23], s44 offen lds\n"
" v_max_f32_e64 v152, v152, v153\n"
" v_mul_f32 v152, -v202, v152\n"
" v_max3_f32 v160, v128, v129, v130\n"
" v_max3_f32 v161, v131, v132, v133\n"
" v_max3_f32 v161, v161, v134, v135\n"
" v_max3_f32 v160, v160, v136, v137\n"
" v_max3_f32 v161, v161, v138, v139\n"
" v_max3_f32 v160, v160, v140, v141\n"
" v_max3_f32 v160, v160, v142, v143\n"
" v_max_f32_e64 v160, v160, v161\n"
" v_mul_f32 v160, -v203, v160\n"
" v_pk_fma_f32 v[112:113], v[112:113], v[202:203], v[152:153] op_sel_hi:[1,0,0]\n"
" v_pk_fma_f32 v[114:115], v[114:115], v[202:203], v[152:153] op_sel_hi:[1,0,0]\n"
" v_pk_fma_f32 v[116:117], v[116:117], v[202:203], v[152:153] op_sel_hi:[1,0,0]\n"
" v_pk_fma_f32 v[118:119], v[118:119], v[202:203], v[152:153] op_sel_hi:[1,0,0]\n"
" v_pk_fma_f32 v[120:121], v[120:121], v[202:203], v[152:153] op_sel_hi:[1,0,0]\n"
" v_pk_fma_f32 v[122:123], v[122:123], v[202:203], v[152:153] op_sel_hi:[1,0,0]\n"
" v_pk_fma_f32 v[124:125], v[124:125], v[202:203], v[152:153] op_sel_hi:[1,0,0]\n"
" v_pk_fma_f32 v[126:127], v[126:127], v[202:203], v[152:153] op_sel_hi:[1,0,0]\n"
" v_exp_f32 v112, v112\n"
" buffer_load_dwordx4 v7, s[20:23], s44 offen offset:1024 lds\n"
" s_add_u32 m0, m0, 8192\n"
" v_exp_f32 v113, v113\n"
" v_exp_f32 v114, v114\n"
" v_exp_f32 v115, v115\n"
" v_exp_f32 v116, v116\n"
" v_exp_f32 v117, v117\n"
" v_exp_f32 v118, v118\n"
" v_exp_f32 v119, v119\n"
" v_exp_f32 v120, v120\n"
" v_exp_f32 v121, v121\n"
" buffer_load_dwordx4 v177, s[20:23], s45 offen lds\n"
" v_exp_f32 v122, v122\n"
" v_exp_f32 v123, v123\n"
" v_exp_f32 v124, v124\n"
" v_exp_f32 v125, v125\n"
" v_exp_f32 v126, v126\n"
" v_exp_f32 v127, v127\n"
" v_pk_fma_f32 v[128:129], v[128:129], v[202:203], v[160:161] op_sel:[0,1,0] op_sel_hi:[1,1,0]\n"
" v_pk_fma_f32 v[130:131], v[130:131], v[202:203], v[160:161] op_sel:[0,1,0] op_sel_hi:[1,1,0]\n"
" v_pk_fma_f32 v[132:133], v[132:133], v[202:203], v[160:161] op_sel:[0,1,0] op_sel_hi:[1,1,0]\n"
" v_pk_fma_f32 v[134:135], v[134:135], v[202:203], v[160:161] op_sel:[0,1,0] op_sel_hi:[1,1,0]\n"
" v_pk_fma_f32 v[136:137], v[136:137], v[202:203], v[160:161] op_sel:[0,1,0] op_sel_hi:[1,1,0]\n"
" v_pk_fma_f32 v[138:139], v[138:139], v[202:203], v[160:161] op_sel:[0,1,0] op_sel_hi:[1,1,0]\n"
" v_pk_fma_f32 v[140:141], v[140:141], v[202:203], v[160:161] op_sel:[0,1,0] op_sel_hi:[1,1,0]\n"
" v_pk_fma_f32 v[142:143], v[142:143], v[202:203], v[160:161] op_sel:[0,1,0] op_sel_hi:[1,1,0]\n"
" buffer_load_dwordx4 v7, s[20:23], s45 offen offset:1024 lds\n"
" s_add_u32 m0, m0, 8192\n"
" v_exp_f32 v128, v128\n"
" v_exp_f32 v129, v129\n"
" v_exp_f32 v130, v130\n"
" v_exp_f32 v131, v131\n"
" v_exp_f32 v132, v132\n"
" v_exp_f32 v133, v133\n"
" v_exp_f32 v134, v134\n"
" v_exp_f32 v135, v135\n"
" v_exp_f32 v136, v136\n"
" v_exp_f32 v137, v137\n"
" buffer_load_dwordx4 v177, s[20:23], s46 offen lds\n"
" v_exp_f32 v138, v138\n"
" v_exp_f32 v139, v139\n"
" v_exp_f32 v140, v140\n"
" v_exp_f32 v141, v141\n"
" v_exp_f32 v142, v142\n"
" v_exp_f32 v143, v143\n"
" v_pk_add_f32 v[152:153], v[112:113], v[114:115]\n"
" v_pk_add_f32 v[154:155], v[116:117], v[118:119]\n"
" v_pk_add_f32 v[156:157], v[120:121], v[122:123]\n"
" v_pk_add_f32 v[158:159], v[124:125], v[126:127]\n"
" v_pk_add_f32 v[152:153], v[152:153], v[154:155]\n"
" v_pk_add_f32 v[156:157], v[156:157], v[158:159]\n"
" buffer_load_dwordx4 v7, s[20:23], s46 offen offset:1024 lds\n"
" s_add_u32 m0, m0, 8192\n"
" v_pk_add_f32 v[152:153], v[152:153], v[156:157]\n"
" v_add_f32 v152, v152, v153\n"
" v_rcp_f32 v152, v152\n"
" v_pk_add_f32 v[160:161], v[128:129], v[130:131]\n"
" buffer_load_dwordx4 v177, s[20:23], s47 offen lds\n"
" buffer_load_dwordx4 v7, s[20:23], s47 offen offset:1024 lds\n"
" s_waitcnt vmcnt(16)\n"
" s_barrier\n"
" s_cmp_eq_u32 s3, 3\n"
" s_cbranch_scc0 .LL_f1\n"
" s_lshl_b32 s51, s2, 7\n"
" s_add_u32 s51, s51, 425984\n"
" v_mov_b32 v198, s48\n"
" v_lshlrev_b32_e64 v199, 2, v0\n"
" s_mov_b32 exec_hi, 0\n"
" buffer_store_dword v198, v199, s[16:19], s51 offen sc1\n"
" s_mov_b64 exec, -1\n"
".LL_f1:\n"
" v_pk_add_f32 v[162:163], v[132:133], v[134:135]\n"
" v_pk_add_f32 v[164:165], v[136:137], v[138:139]\n"
" v_pk_add_f32 v[166:167], v[140:141], v[142:143]\n"
" v_pk_add_f32 v[160:161], v[160:161], v[162:163]\n"
" v_pk_add_f32 v[164:165], v[164:165], v[166:167]\n"
" v_pk_add_f32 v[160:161], v[160:161], v[164:165]\n"
" v_add_f32 v160, v160, v161\n"
" v_rcp_f32 v160, v160\n"
" v_pk_mul_f32 v[112:113], v[112:113], v[152:153] op_sel_hi:[1,0]\n"
" v_pk_mul_f32 v[114:115], v[114:115], v[152:153] op_sel_hi:[1,0]\n"
" v_pk_mul_f32 v[116:117], v[116:117], v[152:153] op_sel_hi:[1,0]\n"
" v_pk_mul_f32 v[118:119], v[118:119], v[152:153] op_sel_hi:[1,0]\n"
" v_pk_mul_f32 v[120:121], v[120:121], v[152:153] op_sel_hi:[1,0]\n"
" v_pk_mul_f32 v[122:123], v[122:123], v[152:153] op_sel_hi:[1,0]\n"
" v_pk_mul_f32 v[124:125], v[124:125], v[152:153] op_sel_hi:[1,0]\n"
" v_pk_mul_f32 v[126:127], v[126:127], v[152:153] op_sel_hi:[1,0]\n"
" v_cvt_pk_bf16_f32 v96, v112, v113\n"
" v_cvt_pk_bf16_f32 v100, v120, v121\n"
" v_cvt_pk_bf16_f32 v97, v114, v115\n"
" v_cvt_pk_bf16_f32 v101, v122, v123\n"
" v_cvt_pk_bf16_f32 v98, v116, v117\n"
" v_cvt_pk_bf16_f32 v102, v124, v125\n"
" v_cvt_pk_bf16_f32 v99, v118, v119\n"
" v_cvt_pk_bf16_f32 v103, v126, v127\n"
" v_permlane16_swap_b32 v96, v100\n"
" v_permlane16_swap_b32 v97, v101\n"
" v_permlane16_swap_b32 v98, v102\n"
" v_permlane16_swap_b32 v99, v103\n"
" v_pk_mul_f32 v[128:129], v[128:129], v[160:161] op_sel_hi:[1,0]\n"
" v_pk_mul_f32 v[130:131], v[130:131], v[160:161] op_sel_hi:[1,0]\n"
" v_pk_mul_f32 v[132:133], v[132:133], v[160:161] op_sel_hi:[1,0]\n"
" v_pk_mul_f32 v[134:135], v[134:135], v[160:161] op_sel_hi:[1,0]\n"
" v_pk_mul_f32 v[136:137], v[136:137], v[160:161] op_sel_hi:[1,0]\n"
" v_pk_mul_f32 v[138:139], v[138:139], v[160:161] op_sel_hi:[1,0]\n"
" v_pk_mul_f32 v[140:141], v[140:141], v[160:161] op_sel_hi:[1,0]\n"
" v_pk_mul_f32 v[142:143], v[142:143], v[160:161] op_sel_hi:[1,0]\n"
" v_mfma_f32_16x16x32_bf16 v[16:19], v[96:99], v[144:147], 0\n"
" v_cvt_pk_bf16_f32 v104, v128, v129\n"
" v_cvt_pk_bf16_f32 v108, v136, v137\n"
" v_mfma_f32_16x16x32_bf16 v[20:23], v[96:99], v[148:151], 0\n"
" v_cvt_pk_bf16_f32 v105, v130, v131\n"
" v_cvt_pk_bf16_f32 v109, v138, v139\n"
" v_mfma_f32_16x16x32_bf16 v[24:27], v[100:103], v[144:147], 0\n"
" v_cvt_pk_bf16_f32 v106, v132, v133\n"
" v_cvt_pk_bf16_f32 v110, v140, v141\n"
" v_mfma_f32_16x16x32_bf16 v[28:31], v[100:103], v[148:151], 0\n"
" v_cvt_pk_bf16_f32 v107, v134, v135\n"
" v_cvt_pk_bf16_f32 v111, v142, v143\n"
" v_permlane16_swap_b32 v104, v108\n"
" v_permlane16_swap_b32 v105, v109\n"
" v_permlane16_swap_b32 v106, v110\n"
" v_permlane16_swap_b32 v107, v111\n"
" s_nop 1\n"
" v_mfma_f32_16x16x32_bf16 v[32:35], v[104:107], v[144:147], 0\n"
" v_mfma_f32_16x16x32_bf16 v[36:39], v[104:107], v[148:151], 0\n"
" v_mfma_f32_16x16x32_bf16 v[40:43], v[108:111], v[144:147], 0\n"
" v_mfma_f32_16x16x32_bf16 v[44:47], v[108:111], v[148:151], 0\n"
" v_cvt_pk_bf16_f32 v112, v16, v17\n"
" v_cvt_pk_bf16_f32 v113, v18, v19\n"
" v_cvt_pk_bf16_f32 v120, v20, v21\n"
" v_cvt_pk_bf16_f32 v121, v22, v23\n"
" v_cvt_pk_bf16_f32 v114, v24, v25\n"
" v_cvt_pk_bf16_f32 v115, v26, v27\n"
" v_cvt_pk_bf16_f32 v122, v28, v29\n"
" v_cvt_pk_bf16_f32 v123, v30, v31\n"
" v_mfma_f32_16x16x32_bf16 v[48:51], v[112:115], v[168:171], 0\n"
" s_nop 0\n"
" v_mfma_f32_16x16x32_bf16 v[52:55], v[120:123], v[168:171], 0\n"
" v_mfma_f32_16x16x32_bf16 v[56:59], v[112:115], v[228:231], 0\n"
" v_mfma_f32_16x16x32_bf16 v[56:59], v[120:123], v[232:235], v[56:59]\n"
" v_cvt_pk_bf16_f32 v116, v32, v33\n"
" v_cvt_pk_bf16_f32 v117, v34, v35\n"
" v_cvt_pk_bf16_f32 v124, v36, v37\n"
" v_cvt_pk_bf16_f32 v125, v38, v39\n"
" v_cvt_pk_bf16_f32 v118, v40, v41\n"
" v_cvt_pk_bf16_f32 v119, v42, v43\n"
" v_cvt_pk_bf16_f32 v126, v44, v45\n"
" v_cvt_pk_bf16_f32 v127, v46, v47\n"
" v_mfma_f32_16x16x32_bf16 v[48:51], v[116:119], v[172:175], v[48:51]\n"
" s_nop 0\n"
" v_mfma_f32_16x16x32_bf16 v[52:55], v[124:127], v[172:175], v[52:55]\n"
" v_mfma_f32_16x16x32_bf16 v[56:59], v[116:119], v[228:231], v[56:59]\n"
" v_mfma_f32_16x16x32_bf16 v[56:59], v[124:127], v[232:235], v[56:59]\n"
" s_nop 3\n"
" v_bfi_b32 v48, s74, v244, v48\n"
" v_bfi_b32 v49, s74, v245, v49\n"
" v_bfi_b32 v50, s74, v246, v50\n"
" v_bfi_b32 v51, s74, v247, v51\n"
" buffer_store_dwordx4 v[48:51], v188, s[16:19], s65 offen sc1\n"
" v_bfi_b32 v52, s74, v244, v52\n"
" v_bfi_b32 v53, s74, v245, v53\n"
" v_bfi_b32 v54, s74, v246, v54\n"
" v_bfi_b32 v55, s74, v247, v55\n"
" buffer_store_dwordx4 v[52:55], v188, s[16:19], s68 offen sc1\n"
" s_mov_b32 exec_lo, 0x00030003\n"
" s_mov_b32 exec_hi, 0x00030003\n"
" v_bfi_b32 v56, s74, v244, v56\n"
" v_bfi_b32 v57, s74, v245, v57\n"
" v_bfi_b32 v58, s74, v246, v58\n"
" v_bfi_b32 v59, s74, v247, v59\n"
" buffer_store_dwordx4 v[56:59], v189, s[16:19], s67 offen sc1\n"
" s_mov_b64 exec, -1\n"
" v_mov_b32 v92, s48\n"
" s_waitcnt vmcnt(3)\n"
" s_barrier\n"
" ds_read_b64_tr_b16 v[96:97], v178 offset:0\n"
" ds_read_b64_tr_b16 v[98:99], v178 offset:512\n"
" ds_read_b128 v[16:19], v201 offset:0\n"
" ds_read_b128 v[20:23], v201 offset:1024\n"
" ds_read_b128 v[24:27], v201 offset:2048\n"
" ds_read_b128 v[28:31], v201 offset:3072\n"
" ds_read_b64_tr_b16 v[100:101], v178 offset:1024\n"
" ds_read_b64_tr_b16 v[102:103], v178 offset:1536\n"
" ds_read_b128 v[32:35], v201 offset:4096\n"
" ds_read_b128 v[36:39], v201 offset:5120\n"
" ds_read_b128 v[40:43], v201 offset:6144\n"
" ds_read_b128 v[44:47], v201 offset:7168\n"
" s_waitcnt lgkmcnt(6)\n"
" v_mfma_f32_16x16x32_bf16 v[64:67], v[96:99], v[16:19], 0\n"
" ds_read_b64_tr_b16 v[104:105], v178 offset:8192\n"
" ds_read_b64_tr_b16 v[106:107], v178 offset:8704\n"
" v_mfma_f32_16x16x32_bf16 v[68:71], v[96:99], v[20:23], 0\n"
" ds_read_b128 v[48:51], v201 offset:8192\n"
" ds_read_b128 v[52:55], v201 offset:9216\n"
" v_mfma_f32_16x16x32_bf16 v[72:75], v[96:99], v[24:27], 0\n"
" ds_read_b128 v[56:59], v201 offset:10240\n"
" ds_read_b128 v[60:63], v201 offset:11264\n"
" v_mfma_f32_16x16x32_bf16 v[76:79], v[96:99], v[28:31], 0\n"
" s_waitcnt lgkmcnt(6)\n"
" v_mfma_f32_16x16x32_bf16 v[64:67], v[100:103], v[32:35], v[64:67]\n"
" ds_read_b64_tr_b16 v[96:97], v178 offset:9216\n"
" ds_read_b64_tr_b16 v[98:99], v178 offset:9728\n"
" v_mfma_f32_16x16x32_bf16 v[68:71], v[100:103], v[36:39], v[68:71]\n"
" ds_read_b128 v[16:19], v201 offset:12288\n"
" ds_read_b128 v[20:23], v201 offset:13312\n"
" v_mfma_f32_16x16x32_bf16 v[72:75], v[100:103], v[40:43], v[72:75]\n"
" ds_read_b128 v[24:27], v201 offset:14336\n"
" ds_read_b128 v[28:31], v201 offset:15360\n"
" v_mfma_f32_16x16x32_bf16 v[76:79], v[100:103], v[44:47], v[76:79]\n"
" s_mov_b32 exec_lo, 0x03ff00ff\n"
" s_mov_b32 exec_hi, 0xff\n"
" buffer_load_dword v92, v238, s[16:19], 0 offen sc1\n"
" s_mov_b64 exec, -1\n"
" s_waitcnt lgkmcnt(6)\n"
" s_nop 0\n"
" v_mfma_f32_16x16x32_bf16 v[64:67], v[104:107], v[48:51], v[64:67]\n"
" ds_read_b64_tr_b16 v[100:101], v178 offset:16384\n"
" ds_read_b64_tr_b16 v[102:103], v178 offset:16896\n"
" v_mfma_f32_16x16x32_bf16 v[68:71], v[104:107], v[52:55], v[68:71]\n"
" ds_read_b128 v[32:35], v201 offset:16384\n"
" ds_read_b128 v[36:39], v201 offset:17408\n"
" v_mfma_f32_16x16x32_bf16 v[72:75], v[104:107], v[56:59], v[72:75]\n"
" ds_read_b128 v[40:43], v201 offset:18432\n"
" ds_read_b128 v[44:47], v201 offset:19456\n"
" v_mfma_f32_16x16x32_bf16 v[76:79], v[104:107], v[60:63], v[76:79]\n"
" s_mov_b32 s67, 14336\n"
" buffer_load_dwordx4 v[212:215], v181, s[16:19], s67 offen sc1\n"
" s_waitcnt lgkmcnt(6)\n"
" v_mfma_f32_16x16x32_bf16 v[64:67], v[96:99], v[16:19], v[64:67]\n"
" ds_read_b64_tr_b16 v[104:105], v178 offset:17408\n"
" ds_read_b64_tr_b16 v[106:107], v178 offset:17920\n"
" v_mfma_f32_16x16x32_bf16 v[68:71], v[96:99], v[20:23], v[68:71]\n"
" ds_read_b128 v[48:51], v201 offset:20480\n"
" ds_read_b128 v[52:55], v201 offset:21504\n"
" v_mfma_f32_16x16x32_bf16 v[72:75], v[96:99], v[24:27], v[72:75]\n"
" ds_read_b128 v[56:59], v201 offset:22528\n"
" ds_read_b128 v[60:63], v201 offset:23552\n"
" v_mfma_f32_16x16x32_bf16 v[76:79], v[96:99], v[28:31], v[76:79]\n"
" s_mov_b32 s67, 26624\n"
" buffer_load_dwordx4 v[216:219], v181, s[16:19], s67 offen sc1\n"
" s_waitcnt lgkmcnt(6)\n"
" v_mfma_f32_16x16x32_bf16 v[64:67], v[100:103], v[32:35], v[64:67]\n"
" ds_read_b64_tr_b16 v[96:97], v178 offset:24576\n"
" ds_read_b64_tr_b16 v[98:99], v178 offset:25088\n"
" v_mfma_f32_16x16x32_bf16 v[68:71], v[100:103], v[36:39], v[68:71]\n"
" ds_read_b128 v[16:19], v201 offset:24576\n"
" ds_read_b128 v[20:23], v201 offset:25600\n"
" v_mfma_f32_16x16x32_bf16 v[72:75], v[100:103], v[40:43], v[72:75]\n"
" ds_read_b128 v[24:27], v201 offset:26624\n"
" ds_read_b128 v[28:31], v201 offset:27648\n"
" v_mfma_f32_16x16x32_bf16 v[76:79], v[100:103], v[44:47], v[76:79]\n"
" s_mov_b32 s67, 38912\n"
" buffer_load_dwordx4 v[220:223], v181, s[16:19], s67 offen sc1\n"
" s_waitcnt lgkmcnt(6)\n"
" v_mfma_f32_16x16x32_bf16 v[64:67], v[104:107], v[48:51], v[64:67]\n"
" ds_read_b64_tr_b16 v[100:101], v178 offset:25600\n"
" ds_read_b64_tr_b16 v[102:103], v178 offset:26112\n"
" v_mfma_f32_16x16x32_bf16 v[68:71], v[104:107], v[52:55], v[68:71]\n"
" ds_read_b128 v[32:35], v201 offset:28672\n"
" ds_read_b128 v[36:39], v201 offset:29696\n"
" v_mfma_f32_16x16x32_bf16 v[72:75], v[104:107], v[56:59], v[72:75]\n"
" ds_read_b128 v[40:43], v201 offset:30720\n"
" ds_read_b128 v[44:47], v201 offset:31744\n"
" v_mfma_f32_16x16x32_bf16 v[76:79], v[104:107], v[60:63], v[76:79]\n"
" s_mov_b32 s67, 51200\n"
" buffer_load_dwordx4 v[224:227], v181, s[16:19], s67 offen sc1\n"
" s_waitcnt lgkmcnt(6)\n"
" v_mfma_f32_16x16x32_bf16 v[64:67], v[96:99], v[16:19], v[64:67]\n"
" ds_read_b64_tr_b16 v[104:105], v178 offset:32768\n"
" ds_read_b64_tr_b16 v[106:107], v178 offset:33280\n"
" v_mfma_f32_16x16x32_bf16 v[68:71], v[96:99], v[20:23], v[68:71]\n"
" ds_read_b128 v[48:51], v201 offset:32768\n"
" ds_read_b128 v[52:55], v201 offset:33792\n"
" v_mfma_f32_16x16x32_bf16 v[72:75], v[96:99], v[24:27], v[72:75]\n"
" ds_read_b128 v[56:59], v201 offset:34816\n"
" ds_read_b128 v[60:63], v201 offset:35840\n"
" v_mfma_f32_16x16x32_bf16 v[76:79], v[96:99], v[28:31], v[76:79]\n"
" s_lshl2_add_u32 s67, s58, s3\n"
" s_lshl_b32 s67, s67, 6\n"
" s_add_u32 s67, s67, 12288\n"
" buffer_load_dwordx4 v[208:211], v193, s[16:19], s67 offen sc1\n"
" s_waitcnt lgkmcnt(6)\n"
" v_mfma_f32_16x16x32_bf16 v[64:67], v[100:103], v[32:35], v[64:67]\n"
" ds_read_b64_tr_b16 v[96:97], v178 offset:33792\n"
" ds_read_b64_tr_b16 v[98:99], v178 offset:34304\n"
" v_mfma_f32_16x16x32_bf16 v[68:71], v[100:103], v[36:39], v[68:71]\n"
" ds_read_b128 v[16:19], v201 offset:36864\n"
" ds_read_b128 v[20:23], v201 offset:37888\n"
" v_mfma_f32_16x16x32_bf16 v[72:75], v[100:103], v[40:43], v[72:75]\n"
" ds_read_b128 v[24:27], v201 offset:38912\n"
" ds_read_b128 v[28:31], v201 offset:39936\n"
" v_mfma_f32_16x16x32_bf16 v[76:79], v[100:103], v[44:47], v[76:79]\n"
" s_waitcnt lgkmcnt(6)\n"
" v_mfma_f32_16x16x32_bf16 v[64:67], v[104:107], v[48:51], v[64:67]\n"
" ds_read_b64_tr_b16 v[100:101], v178 offset:40960\n"
" ds_read_b64_tr_b16 v[102:103], v178 offset:41472\n"
" v_mfma_f32_16x16x32_bf16 v[68:71], v[104:107], v[52:55], v[68:71]\n"
" ds_read_b128 v[32:35], v201 offset:40960\n"
" ds_read_b128 v[36:39], v201 offset:41984\n"
" v_mfma_f32_16x16x32_bf16 v[72:75], v[104:107], v[56:59], v[72:75]\n"
" ds_read_b128 v[40:43], v201 offset:43008\n"
" ds_read_b128 v[44:47], v201 offset:44032\n"
" v_mfma_f32_16x16x32_bf16 v[76:79], v[104:107], v[60:63], v[76:79]\n"
" s_waitcnt lgkmcnt(6)\n"
" v_mfma_f32_16x16x32_bf16 v[64:67], v[96:99], v[16:19], v[64:67]\n"
" ds_read_b64_tr_b16 v[104:105], v178 offset:41984\n"
" ds_read_b64_tr_b16 v[106:107], v178 offset:42496\n"
" v_mfma_f32_16x16x32_bf16 v[68:71], v[96:99], v[20:23], v[68:71]\n"
" ds_read_b128 v[48:51], v201 offset:45056\n"
" ds_read_b128 v[52:55], v201 offset:46080\n"
" v_mfma_f32_16x16x32_bf16 v[72:75], v[96:99], v[24:27], v[72:75]\n"
" ds_read_b128 v[56:59], v201 offset:47104\n"
" ds_read_b128 v[60:63], v201 offset:48128\n"
" v_mfma_f32_16x16x32_bf16 v[76:79], v[96:99], v[28:31], v[76:79]\n"
".Lf1_chk:\n"
" s_waitcnt vmcnt(5)\n"
" v_cmp_ne_u32 vcc, s48, v92\n"
" s_nop 0\n"
" s_cbranch_vccnz .Lf1_slow\n"
".Lf1_ok:\n"
" s_barrier\n"
" buffer_load_dwordx4 v[80:83], v1, s[16:19], s69 offen offset:0 sc1\n"
" buffer_load_dwordx4 v[84:87], v1, s[16:19], s69 offen offset:1024 sc1\n"
" s_waitcnt lgkmcnt(6)\n"
" v_mfma_f32_16x16x32_bf16 v[64:67], v[100:103], v[32:35], v[64:67]\n"
" ds_read_b64_tr_b16 v[96:97], v178 offset:49152\n"
" ds_read_b64_tr_b16 v[98:99], v178 offset:49664\n"
" v_mfma_f32_16x16x32_bf16 v[68:71], v[100:103], v[36:39], v[68:71]\n"
" ds_read_b128 v[16:19], v201 offset:49152\n"
" ds_read_b128 v[20:23], v201 offset:50176\n"
" v_mfma_f32_16x16x32_bf16 v[72:75], v[100:103], v[40:43], v[72:75]\n"
" ds_read_b128 v[24:27], v201 offset:51200\n"
" ds_read_b128 v[28:31], v201 offset:52224\n"
" v_mfma_f32_16x16x32_bf16 v[76:79], v[100:103], v[44:47], v[76:79]\n"
" s_mov_b32 m0, s72\n"
" s_nop 0\n"
" buffer_load_dwordx4 v196, s[32:35], s90 offen offset:0 lds\n"
" s_waitcnt lgkmcnt(6)\n"
" v_mfma_f32_16x16x32_bf16 v[64:67], v[104:107], v[48:51], v[64:67]\n"
" ds_read_b64_tr_b16 v[100:101], v178 offset:50176\n"
" ds_read_b64_tr_b16 v[102:103], v178 offset:50688\n"
" v_mfma_f32_16x16x32_bf16 v[68:71], v[104:107], v[52:55], v[68:71]\n"
" ds_read_b128 v[32:35], v201 offset:53248\n"
" ds_read_b128 v[36:39], v201 offset:54272\n"
" v_mfma_f32_16x16x32_bf16 v[72:75], v[104:107], v[56:59], v[72:75]\n"
" ds_read_b128 v[40:43], v201 offset:55296\n"
" ds_read_b128 v[44:47], v201 offset:56320\n"
" v_mfma_f32_16x16x32_bf16 v[76:79], v[104:107], v[60:63], v[76:79]\n"
" s_waitcnt lgkmcnt(6)\n"
" buffer_load_dwordx4 v197, s[32:35], s90 offen offset:1024 lds\n"
" s_add_u32 m0, m0, 2048\n"
" v_mfma_f32_16x16x32_bf16 v[64:67], v[96:99], v[16:19], v[64:67]\n"
" ds_read_b64_tr_b16 v[104:105], v178 offset:57344\n"
" ds_read_b64_tr_b16 v[106:107], v178 offset:57856\n"
" v_mfma_f32_16x16x32_bf16 v[68:71], v[96:99], v[20:23], v[68:71]\n"
" ds_read_b128 v[48:51], v201 offset:57344\n"
" ds_read_b128 v[52:55], v201 offset:58368\n"
" v_mfma_f32_16x16x32_bf16 v[72:75], v[96:99], v[24:27], v[72:75]\n"
" ds_read_b128 v[56:59], v201 offset:59392\n"
" ds_read_b128 v[60:63], v201 offset:60416\n"
" v_mfma_f32_16x16x32_bf16 v[76:79], v[96:99], v[28:31], v[76:79]\n"
" s_waitcnt lgkmcnt(6)\n"
" buffer_load_dwordx4 v196, s[32:35], s91 offen offset:0 lds\n"
" v_mfma_f32_16x16x32_bf16 v[64:67], v[100:103], v[32:35], v[64:67]\n"
" ds_read_b64_tr_b16 v[96:97], v178 offset:58368\n"
" ds_read_b64_tr_b16 v[98:99], v178 offset:58880\n"
" v_mfma_f32_16x16x32_bf16 v[68:71], v[100:103], v[36:39], v[68:71]\n"
" ds_read_b128 v[16:19], v201 offset:61440\n"
" ds_read_b128 v[20:23], v201 offset:62464\n"
" v_mfma_f32_16x16x32_bf16 v[72:75], v[100:103], v[40:43], v[72:75]\n"
" ds_read_b128 v[24:27], v201 offset:63488\n"
" ds_read_b128 v[28:31], v201 offset:64512\n"
" v_mfma_f32_16x16x32_bf16 v[76:79], v[100:103], v[44:47], v[76:79]\n"
" s_waitcnt lgkmcnt(6)\n"
" buffer_load_dwordx4 v197, s[32:35], s91 offen offset:1024 lds\n"
" v_mfma_f32_16x16x32_bf16 v[64:67], v[104:107], v[48:51], v[64:67]\n"
" v_mfma_f32_16x16x32_bf16 v[68:71], v[104:107], v[52:55], v[68:71]\n"
" v_mfma_f32_16x16x32_bf16 v[72:75], v[104:107], v[56:59], v[72:75]\n"
" v_mfma_f32_16x16x32_bf16 v[76:79], v[104:107], v[60:63], v[76:79]\n"
" s_add_u32 s95, s80, 0\n"
" buffer_load_dwordx4 a[0:3], v1, s[16:19], s95 offen offset:0 sc1\n"
" s_waitcnt lgkmcnt(0)\n"
" v_mfma_f32_16x16x32_bf16 v[64:67], v[96:99], v[16:19], v[64:67]\n"
" v_mfma_f32_16x16x32_bf16 v[68:71], v[96:99], v[20:23], v[68:71]\n"
" v_mfma_f32_16x16x32_bf16 v[72:75], v[96:99], v[24:27], v[72:75]\n"
" v_mfma_f32_16x16x32_bf16 v[76:79], v[96:99], v[28:31], v[76:79]\n"
" buffer_load_dwordx4 a[12:15], v1, s[16:19], s95 offen offset:1024 sc1\n"
".Lkvp_chk:\n"
" s_waitcnt vmcnt(7)\n"
" v_xor_b32 v80, s52, v80\n"
" v_xor_b32 v81, s53, v81\n"
" v_xor_b32 v82, s54, v82\n"
" v_xor_b32 v83, s55, v83\n"
" s_waitcnt vmcnt(6)\n"
" v_xor_b32 v84, s52, v84\n"
" v_xor_b32 v85, s53, v85\n"
" v_xor_b32 v86, s54, v86\n"
" v_xor_b32 v87, s55, v87\n"
" v_or_b32 v88, v80, v81\n"
" v_or3_b32 v88, v88, v82, v83\n"
" v_or3_b32 v88, v88, v84, v85\n"
" v_or3_b32 v88, v88, v86, v87\n"
" v_and_b32 v88, s74, v88\n"
" v_cmp_ne_u32 vcc, 0, v88\n"
" s_nop 0\n"
" s_cbranch_vccnz .Lkvp_slow\n"
".Lkvp_ok:\n"
" v_pk_add_f32 v[80:81], v[80:81], v[84:85]\n"
" v_pk_add_f32 v[82:83], v[82:83], v[86:87]\n"
" s_nop 0\n"
" v_permlane32_swap_b32 v80, v81\n"
" v_permlane32_swap_b32 v82, v83\n"
" v_add_f32 v80, v80, v81\n"
" v_add_f32 v82, v82, v83\n"
" s_nop 1\n"
" v_permlane16_swap_b32 v80, v82\n"
" v_add_f32 v80, v80, v82\n"
" s_nop 1\n"
" v_add_f32_dpp v80, v80, v80 row_ror:8 row_mask:0xf bank_mask:0xf\n"
" s_nop 1\n"
" v_add_f32_dpp v80, v80, v80 row_ror:4 row_mask:0xf bank_mask:0xf\n"
" s_mov_b32 exec_lo, 0x000f000f\n"
" s_mov_b32 exec_hi, 0x000f000f\n"
" v_bfi_b32 v80, s74, v191, v80\n"
" buffer_store_dword v80, v190, s[16:19], s70 offen sc1\n"
" s_mov_b64 exec, -1\n"
" s_barrier\n"
" s_lshl_b32 s67, s59, 12\n"
" s_add_u32 s67, s67, 163840\n"
" buffer_load_dwordx4 v[16:19], v1, s[16:19], s67 offen offset:0 sc1\n"
" v_cvt_pk_bf16_f32 v96, v64, v65\n"
" v_cvt_pk_bf16_f32 v97, v66, v67\n"
" v_cvt_pk_bf16_f32 v98, v68, v69\n"
" v_cvt_pk_bf16_f32 v99, v70, v71\n"
" v_cvt_pk_bf16_f32 v100, v72, v73\n"
" v_cvt_pk_bf16_f32 v101, v74, v75\n"
" v_cvt_pk_bf16_f32 v102, v76, v77\n"
" v_cvt_pk_bf16_f32 v103, v78, v79\n"
" v_permlane16_swap_b32 v96, v98\n"
" v_permlane16_swap_b32 v97, v99\n"
" buffer_load_dwordx4 v[20:23], v1, s[16:19], s67 offen offset:1024 sc1\n"
" v_permlane16_swap_b32 v100, v102\n"
" v_permlane16_swap_b32 v101, v103\n"
" v_mfma_f32_32x32x16_bf16 v[112:127], v[12:15], v[96:99], 0\n"
" s_nop 0\n"
" v_mfma_f32_32x32x16_bf16 v[128:143], v[12:15], v[100:103], 0\n"
" ; fm_gap\n"
" s_nop 9\n"
" v_max3_f32 v152, v112, v113, v114\n"
" v_max3_f32 v153, v115, v116, v117\n"
" v_max3_f32 v153, v153, v118, v119\n"
" v_max3_f32 v152, v152, v120, v121\n"
" buffer_load_dwordx4 v[24:27], v1, s[16:19], s67 offen offset:2048 sc1\n"
" v_max3_f32 v153, v153, v122, v123\n"
" v_max3_f32 v152, v152, v124, v125\n"
" v_max3_f32 v152, v152, v126, v127\n"
" v_max_f32_e64 v152, v152, v153\n"
" v_mul_f32 v152, -v202, v152\n"
" v_max3_f32 v160, v128, v129, v130\n"
" v_max3_f32 v161, v131, v132, v133\n"
" v_max3_f32 v161, v161, v134, v135\n"
" v_max3_f32 v160, v160, v136, v137\n"
" v_max3_f32 v161, v161, v138, v139\n"
" v_max3_f32 v160, v160, v140, v141\n"
" v_max3_f32 v160, v160, v142, v143\n"
" v_max_f32_e64 v160, v160, v161\n"
" buffer_load_dwordx4 v[28:31], v1, s[16:19], s67 offen offset:3072 sc1\n"
" v_mul_f32 v160, -v203, v160\n"
" v_pk_fma_f32 v[112:113], v[112:113], v[202:203], v[152:153] op_sel_hi:[1,0,0]\n"
" v_pk_fma_f32 v[114:115], v[114:115], v[202:203], v[152:153] op_sel_hi:[1,0,0]\n"
" v_pk_fma_f32 v[116:117], v[116:117], v[202:203], v[152:153] op_sel_hi:[1,0,0]\n"
" v_pk_fma_f32 v[118:119], v[118:119], v[202:203], v[152:153] op_sel_hi:[1,0,0]\n"
" v_pk_fma_f32 v[120:121], v[120:121], v[202:203], v[152:153] op_sel_hi:[1,0,0]\n"
" v_pk_fma_f32 v[122:123], v[122:123], v[202:203], v[152:153] op_sel_hi:[1,0,0]\n"
" v_pk_fma_f32 v[124:125], v[124:125], v[202:203], v[152:153] op_sel_hi:[1,0,0]\n"
" v_pk_fma_f32 v[126:127], v[126:127], v[202:203], v[152:153] op_sel_hi:[1,0,0]\n"
" v_exp_f32 v112, v112\n"
" v_exp_f32 v113, v113\n"
" v_exp_f32 v114, v114\n"
" s_lshl_b32 s65, s71, 3\n"
" s_add_u32 s66, s71, s72\n"
" s_add_u32 m0, s66, 4294901760\n"
" v_exp_f32 v115, v115\n"
" v_exp_f32 v116, v116\n"
" v_exp_f32 v117, v117\n"
" v_exp_f32 v118, v118\n"
" v_exp_f32 v119, v119\n"
" buffer_load_dwordx4 v8, s[4:7], s65 offen offset:0 lds\n"
" v_exp_f32 v120, v120\n"
" v_exp_f32 v121, v121\n"
" v_exp_f32 v122, v122\n"
" v_exp_f32 v123, v123\n"
" v_exp_f32 v124, v124\n"
" v_exp_f32 v125, v125\n"
" v_exp_f32 v126, v126\n"
" buffer_load_dwordx4 v9, s[4:7], s65 offen offset:1024 lds\n"
" v_exp_f32 v127, v127\n"
" v_pk_fma_f32 v[128:129], v[128:129], v[202:203], v[160:161] op_sel:[0,1,0] op_sel_hi:[1,1,0]\n"
" v_pk_fma_f32 v[130:131], v[130:131], v[202:203], v[160:161] op_sel:[0,1,0] op_sel_hi:[1,1,0]\n"
" v_pk_fma_f32 v[132:133], v[132:133], v[202:203], v[160:161] op_sel:[0,1,0] op_sel_hi:[1,1,0]\n"
" v_pk_fma_f32 v[134:135], v[134:135], v[202:203], v[160:161] op_sel:[0,1,0] op_sel_hi:[1,1,0]\n"
" v_pk_fma_f32 v[136:137], v[136:137], v[202:203], v[160:161] op_sel:[0,1,0] op_sel_hi:[1,1,0]\n"
" v_pk_fma_f32 v[138:139], v[138:139], v[202:203], v[160:161] op_sel:[0,1,0] op_sel_hi:[1,1,0]\n"
" v_pk_fma_f32 v[140:141], v[140:141], v[202:203], v[160:161] op_sel:[0,1,0] op_sel_hi:[1,1,0]\n"
" v_pk_fma_f32 v[142:143], v[142:143], v[202:203], v[160:161] op_sel:[0,1,0] op_sel_hi:[1,1,0]\n"
" v_exp_f32 v128, v128\n"
" v_exp_f32 v129, v129\n"
" buffer_load_dwordx4 v10, s[4:7], s65 offen offset:2048 lds\n"
" v_exp_f32 v130, v130\n"
" v_exp_f32 v131, v131\n"
" v_exp_f32 v132, v132\n"
" v_exp_f32 v133, v133\n"
" v_exp_f32 v134, v134\n"
" v_exp_f32 v135, v135\n"
" v_exp_f32 v136, v136\n"
" buffer_load_dwordx4 v11, s[4:7], s65 offen offset:3072 lds\n"
" s_add_u32 s71, s71, 16384\n"
" s_and_b32 s71, s71, 0xffff\n"
" s_lshl_b32 s65, s71, 3\n"
" s_add_u32 s66, s71, s72\n"
" s_add_u32 m0, s66, 4294901760\n"
" v_exp_f32 v137, v137\n"
" v_exp_f32 v138, v138\n"
" v_exp_f32 v139, v139\n"
" v_exp_f32 v140, v140\n"
" v_exp_f32 v141, v141\n"
" buffer_load_dwordx4 v8, s[4:7], s65 offen offset:0 lds\n"
" v_exp_f32 v142, v142\n"
" v_exp_f32 v143, v143\n"
" v_pk_add_f32 v[152:153], v[112:113], v[114:115]\n"
" v_pk_add_f32 v[154:155], v[116:117], v[118:119]\n"
" v_pk_add_f32 v[156:157], v[120:121], v[122:123]\n"
" v_pk_add_f32 v[158:159], v[124:125], v[126:127]\n"
" v_pk_add_f32 v[152:153], v[152:153], v[154:155]\n"
" v_pk_add_f32 v[156:157], v[156:157], v[158:159]\n"
" v_pk_add_f32 v[152:153], v[152:153], v[156:157]\n"
" v_add_f32 v152, v152, v153\n"
" buffer_load_dwordx4 v9, s[4:7], s65 offen offset:1024 lds\n"
" v_rcp_f32 v152, v152\n"
" v_pk_add_f32 v[160:161], v[128:129], v[130:131]\n"
" v_pk_add_f32 v[162:163], v[132:133], v[134:135]\n"
" v_pk_add_f32 v[164:165], v[136:137], v[138:139]\n"
" v_pk_add_f32 v[166:167], v[140:141], v[142:143]\n"
" v_pk_add_f32 v[160:161], v[160:161], v[162:163]\n"
" v_pk_add_f32 v[164:165], v[164:165], v[166:167]\n"
" v_pk_add_f32 v[160:161], v[160:161], v[164:165]\n"
" v_add_f32 v160, v160, v161\n"
" v_rcp_f32 v160, v160\n"
" v_pk_mul_f32 v[112:113], v[112:113], v[152:153] op_sel_hi:[1,0]\n"
" v_pk_mul_f32 v[114:115], v[114:115], v[152:153] op_sel_hi:[1,0]\n"
" buffer_load_dwordx4 v10, s[4:7], s65 offen offset:2048 lds\n"
" v_pk_mul_f32 v[116:117], v[116:117], v[152:153] op_sel_hi:[1,0]\n"
" v_pk_mul_f32 v[118:119], v[118:119], v[152:153] op_sel_hi:[1,0]\n"
" v_pk_mul_f32 v[120:121], v[120:121], v[152:153] op_sel_hi:[1,0]\n"
" v_pk_mul_f32 v[122:123], v[122:123], v[152:153] op_sel_hi:[1,0]\n"
" v_pk_mul_f32 v[124:125], v[124:125], v[152:153] op_sel_hi:[1,0]\n"
" v_pk_mul_f32 v[126:127], v[126:127], v[152:153] op_sel_hi:[1,0]\n"
" v_cvt_pk_bf16_f32 v96, v112, v113\n"
" v_cvt_pk_bf16_f32 v100, v120, v121\n"
" v_cvt_pk_bf16_f32 v97, v114, v115\n"
" v_cvt_pk_bf16_f32 v101, v122, v123\n"
" v_cvt_pk_bf16_f32 v98, v116, v117\n"
" v_cvt_pk_bf16_f32 v102, v124, v125\n"
" v_cvt_pk_bf16_f32 v99, v118, v119\n"
" v_cvt_pk_bf16_f32 v103, v126, v127\n"
" v_permlane16_swap_b32 v96, v100\n"
" v_permlane16_swap_b32 v97, v101\n"
" v_permlane16_swap_b32 v98, v102\n"
" s_lshl_b32 s67, s59, 11\n"
" s_add_u32 s67, s67, 65536\n"
" buffer_load_dwordx4 v[32:35], v194, s[16:19], s67 offen offset:0 sc1\n"
" s_nop 0\n"
" buffer_load_dwordx4 v[36:39], v194, s[16:19], s67 offen offset:256 sc1\n"
" v_permlane16_swap_b32 v99, v103\n"
" v_pk_mul_f32 v[128:129], v[128:129], v[160:161] op_sel_hi:[1,0]\n"
" v_pk_mul_f32 v[130:131], v[130:131], v[160:161] op_sel_hi:[1,0]\n"
" v_pk_mul_f32 v[132:133], v[132:133], v[160:161] op_sel_hi:[1,0]\n"
" v_pk_mul_f32 v[134:135], v[134:135], v[160:161] op_sel_hi:[1,0]\n"
" v_pk_mul_f32 v[136:137], v[136:137], v[160:161] op_sel_hi:[1,0]\n"
" v_pk_mul_f32 v[138:139], v[138:139], v[160:161] op_sel_hi:[1,0]\n"
" v_pk_mul_f32 v[140:141], v[140:141], v[160:161] op_sel_hi:[1,0]\n"
" v_pk_mul_f32 v[142:143], v[142:143], v[160:161] op_sel_hi:[1,0]\n"
" v_cvt_pk_bf16_f32 v104, v128, v129\n"
" buffer_load_dwordx4 v11, s[4:7], s65 offen offset:3072 lds\n"
" s_add_u32 s71, s71, 16384\n"
" s_and_b32 s71, s71, 0xffff\n"
" s_lshl_b32 s65, s71, 3\n"
" s_add_u32 s66, s71, s72\n"
" s_add_u32 m0, s66, 4294901760\n"
" v_cvt_pk_bf16_f32 v108, v136, v137\n"
" v_cvt_pk_bf16_f32 v105, v130, v131\n"
" v_cvt_pk_bf16_f32 v109, v138, v139\n"
" v_cvt_pk_bf16_f32 v106, v132, v133\n"
" v_cvt_pk_bf16_f32 v110, v140, v141\n"
" v_cvt_pk_bf16_f32 v107, v134, v135\n"
" v_cvt_pk_bf16_f32 v111, v142, v143\n"
" v_permlane16_swap_b32 v104, v108\n"
" buffer_load_dwordx4 v8, s[4:7], s65 offen offset:0 lds\n"
" v_permlane16_swap_b32 v105, v109\n"
" v_permlane16_swap_b32 v106, v110\n"
" v_permlane16_swap_b32 v107, v111\n"
" buffer_load_dwordx4 v9, s[4:7], s65 offen offset:1024 lds\n"
" buffer_load_dwordx4 v10, s[4:7], s65 offen offset:2048 lds\n"
" buffer_load_dwordx4 v11, s[4:7], s65 offen offset:3072 lds\n"
" s_add_u32 s71, s71, 16384\n"
" s_and_b32 s71, s71, 0xffff\n"
" s_lshl_b32 s65, s71, 3\n"
" s_add_u32 s66, s71, s72\n"
" s_add_u32 m0, s66, 4294901760\n"
".Lkzp_chk:\n"
" s_waitcnt vmcnt(17)\n"
" v_xor_b32 v16, s52, v16\n"
" v_xor_b32 v17, s53, v17\n"
" v_xor_b32 v18, s54, v18\n"
" v_xor_b32 v19, s55, v19\n"
" s_waitcnt vmcnt(16)\n"
" v_xor_b32 v20, s52, v20\n"
" v_xor_b32 v21, s53, v21\n"
" v_xor_b32 v22, s54, v22\n"
" v_xor_b32 v23, s55, v23\n"
" s_waitcnt vmcnt(15)\n"
" v_xor_b32 v24, s52, v24\n"
" v_xor_b32 v25, s53, v25\n"
" v_xor_b32 v26, s54, v26\n"
" v_xor_b32 v27, s55, v27\n"
" s_waitcnt vmcnt(14)\n"
" v_xor_b32 v28, s52, v28\n"
" v_xor_b32 v29, s53, v29\n"
" v_xor_b32 v30, s54, v30\n"
" v_xor_b32 v31, s55, v31\n"
" v_or_b32_e64 v13, v16, v17\n"
" v_or3_b32 v13, v13, v18, v19\n"
" v_or3_b32 v13, v13, v20, v21\n"
" v_or3_b32 v13, v13, v22, v23\n"
" v_or3_b32 v13, v13, v24, v25\n"
" v_or3_b32 v13, v13, v26, v27\n"
" v_or3_b32 v13, v13, v28, v29\n"
" v_or3_b32 v13, v13, v30, v31\n"
" v_and_b32 v13, s74, v13\n"
" v_cmp_ne_u32 vcc, 0, v13\n"
" s_nop 0\n"
" s_cbranch_vccnz .Lkzp_slow\n"
".Lkzp_ok:\n"
" buffer_load_dwordx4 v8, s[4:7], s65 offen offset:0 lds\n"
" v_pk_add_f32 v[16:17], v[16:17], v[20:21]\n"
" v_pk_add_f32 v[24:25], v[24:25], v[28:29]\n"
" v_pk_add_f32 v[18:19], v[18:19], v[22:23]\n"
" v_pk_add_f32 v[26:27], v[26:27], v[30:31]\n"
" v_pk_add_f32 v[16:17], v[16:17], v[24:25]\n"
" v_pk_add_f32 v[18:19], v[18:19], v[26:27]\n"
" v_add_u32 v13, s100, v190\n"
" buffer_load_dwordx4 v9, s[4:7], s65 offen offset:1024 lds\n"
" v_permlane32_swap_b32 v16, v17\n"
" v_permlane32_swap_b32 v18, v19\n"
" v_add_f32 v16, v16, v17\n"
" v_add_f32_e64 v18, v18, v19\n"
" buffer_load_dwordx4 v10, s[4:7], s65 offen offset:2048 lds\n"
" s_nop 0\n"
" v_permlane16_swap_b32 v16, v18\n"
" v_add_f32 v16, v16, v18\n"
" s_nop 1\n"
" v_add_f32_dpp v16, v16, v16 row_ror:8 row_mask:0xf bank_mask:0xf\n"
" ds_write_b32 v13, v16\n"
" buffer_load_dwordx4 v11, s[4:7], s65 offen offset:3072 lds\n"
" ds_read_b128 v[40:43], v195 offset:0\n"
" ds_read_b128 v[44:47], v195 offset:32\n"
" v_mov_b32 v88, 0x358637bd\n"
" v_mov_b32 v89, 0x358637bd\n"
" v_mov_b32 v90, 0x358637bd\n"
" v_mov_b32 v91, 0x358637bd\n"
".Lkvr_chk:\n"
" s_waitcnt vmcnt(10)\n"
" v_xor_b32 v32, s52, v32\n"
" v_xor_b32 v33, s53, v33\n"
" v_xor_b32 v34, s54, v34\n"
" v_xor_b32 v35, s55, v35\n"
" s_waitcnt vmcnt(9)\n"
" v_xor_b32 v36, s52, v36\n"
" v_xor_b32 v37, s53, v37\n"
" v_xor_b32 v38, s54, v38\n"
" v_xor_b32 v39, s55, v39\n"
" v_or_b32 v13, v32, v33\n"
" v_or3_b32 v13, v13, v34, v35\n"
" v_or3_b32 v13, v13, v36, v37\n"
" v_or3_b32 v13, v13, v38, v39\n"
" v_and_b32 v13, s74, v13\n"
" v_cmp_ne_u32_e64 vcc, 0, v13\n"
" s_nop 0\n"
" s_cbranch_vccnz .Lkvr_slow\n"
".Lkvr_ok:\n"
" s_waitcnt lgkmcnt(1)\n"
" v_cvt_pk_bf16_f32 v52, v40, v41\n"
" v_cvt_pk_bf16_f32 v53, v42, v43\n"
" s_waitcnt lgkmcnt(0)\n"
" v_cvt_pk_bf16_f32 v54, v44, v45\n"
" v_cvt_pk_bf16_f32 v55, v46, v47\n"
" v_cvt_pk_bf16_f32 v48, v32, v33\n"
" v_cvt_pk_bf16_f32 v49, v34, v35\n"
" v_cvt_pk_bf16_f32 v50, v36, v37\n"
" v_cvt_pk_bf16_f32 v51, v38, v39\n"
" v_mfma_f32_16x16x32_bf16 v[128:131], v[52:55], v[96:99], v[88:91]\n"
" v_mfma_f32_16x16x32_bf16 v[132:135], v[52:55], v[100:103], v[88:91]\n"
" v_mfma_f32_16x16x32_bf16 v[136:139], v[52:55], v[104:107], v[88:91]\n"
" v_mfma_f32_16x16x32_bf16 v[140:143], v[52:55], v[108:111], v[88:91]\n"
" v_mfma_f32_16x16x32_bf16 v[112:115], v[48:51], v[96:99], 0\n"
" v_mfma_f32_16x16x32_bf16 v[116:119], v[48:51], v[100:103], 0\n"
" v_mfma_f32_16x16x32_bf16 v[120:123], v[48:51], v[104:107], 0\n"
" v_mfma_f32_16x16x32_bf16 v[124:127], v[48:51], v[108:111], 0\n"
" s_nop 0\n"
" v_rcp_f32 v128, v128\n"
" v_rcp_f32 v132, v132\n"
" v_rcp_f32 v136, v136\n"
" v_rcp_f32_e64 v140, v140\n"
" v_pk_mul_f32 v[112:113], v[112:113], v[128:129] op_sel_hi:[1,0]\n"
" v_pk_mul_f32 v[114:115], v[114:115], v[128:129] op_sel_hi:[1,0]\n"
" v_cvt_pk_bf16_f32 v56, v112, v113\n"
" v_cvt_pk_bf16_f32 v57, v114, v115\n"
" v_pk_mul_f32 v[116:117], v[116:117], v[132:133] op_sel_hi:[1,0]\n"
" v_pk_mul_f32 v[118:119], v[118:119], v[132:133] op_sel_hi:[1,0]\n"
" v_cvt_pk_bf16_f32 v58, v116, v117\n"
" v_cvt_pk_bf16_f32 v59, v118, v119\n"
" v_pk_mul_f32 v[120:121], v[120:121], v[136:137] op_sel_hi:[1,0]\n"
" v_pk_mul_f32 v[122:123], v[122:123], v[136:137] op_sel_hi:[1,0]\n"
" v_cvt_pk_bf16_f32 v60, v120, v121\n"
" v_cvt_pk_bf16_f32 v61, v122, v123\n"
" v_pk_mul_f32 v[124:125], v[124:125], v[140:141] op_sel_hi:[1,0]\n"
" v_pk_mul_f32 v[126:127], v[126:127], v[140:141] op_sel_hi:[1,0]\n"
" v_cvt_pk_bf16_f32 v62, v124, v125\n"
" v_cvt_pk_bf16_f32 v63, v126, v127\n"
" v_permlane16_swap_b32 v56, v58\n"
" v_permlane16_swap_b32 v57, v59\n"
" v_permlane16_swap_b32 v60, v62\n"
" v_permlane16_swap_b32 v61, v63\n"
" buffer_store_dwordx4 v[56:59], v241, s[16:19], s85 offen sc1\n"
" buffer_store_dwordx4 v[60:63], v239, s[16:19], s85 offen sc1\n"
" v_bfe_u32 v9, v0, 2, 1\n"
" v_and_b32 v11, 0x30, v1\n"
" s_lshl2_add_u32 s65, s57, s3\n"
" s_lshl_b32 s65, s65, 7\n"
" s_add_u32 s65, s65, s58\n"
" s_lshl_b32 s65, s65, 7\n"
" s_add_u32 s66, s65, 7168\n"
" s_lshl_b32 m0, s3, 11\n"
" v_and_b32 v10, 7, v0\n"
" v_xor_b32 v10, v10, v5\n"
" v_lshlrev_b32 v10, 4, v10\n"
" v_lshl_add_u32 v10, v5, 10, v10\n"
" buffer_load_dwordx4 v10, s[12:15], s65 offen lds\n"
" buffer_load_dwordx4 v10, s[12:15], s66 offen offset:1024 lds\n"
" buffer_load_dwordx4 a[24:27], v1, s[16:19], s95 offen offset:2048 sc1\n"
" v_mov_b32 v237, s48\n"
" v_lshrrev_b32 v14, 5, v0\n"
" v_lshl_add_u32 v15, v9, 8, v190\n"
" v_and_b32 v15, 0x13f, v15\n"
" s_lshl_b32 s65, s3, 6\n"
" s_add_u32 s65, s65, 160256\n"
" v_add_u32_e64 v15, s65, v15\n"
" v_lshl_add_u32 v9, v9, 3, v11\n"
" v_bfe_u32 v10, v0, 3, 2\n"
" buffer_load_dwordx4 a[36:39], v1, s[16:19], s95 offen offset:3072 sc1\n"
" v_lshl_add_u32 v9, v10, 6, v9\n"
" v_lshl_add_u32 v9, v14, 10, v9\n"
" s_lshl_b32 s65, s3, 8\n"
" s_add_u32 s65, s65, 158208\n"
" v_add_u32 v9, s65, v9\n"
" v_and_b32 v11, 7, v3\n"
" v_xor_b32 v11, v11, v14\n"
" v_lshlrev_b32 v11, 4, v11\n"
" v_lshl_add_u32 v11, v3, 7, v11\n"
" s_add_u32 s95, s80, 16384\n"
" buffer_load_dwordx4 a[4:7], v1, s[16:19], s95 offen offset:0 sc1\n"
" v_and_b32 v10, 1, v2\n"
" v_lshl_add_u32 v11, v10, 3, v11\n"
" v_lshl_add_u32 v11, s3, 11, v11\n"
" s_lshl_b32 s65, s3, 5\n"
" v_xor_b32 v4, s65, v176\n"
" v_xor_b32 v5, 32, v4\n"
" v_xor_b32 v6, 64, v4\n"
" buffer_load_dwordx4 a[16:19], v1, s[16:19], s95 offen offset:1024 sc1\n"
" v_xor_b32 v7, 96, v4\n"
" v_lshlrev_b32 v14, 4, v2\n"
" v_add_u32 v13, 160256, v14\n"
" s_lshl_b32 s65, s58, 7\n"
" s_add_u32 s65, s65, 159232\n"
" v_add_u32_e64 v14, s65, v14\n"
" ds_read_b64_tr_b16 a[192:193], v192 offset:0\n"
" buffer_load_dwordx4 a[28:31], v1, s[16:19], s95 offen offset:2048 sc1\n"
" ds_read_b64_tr_b16 a[194:195], v192 offset:1024\n"
" ds_read_b64_tr_b16 a[196:197], v192 offset:32\n"
" ds_read_b64_tr_b16 a[198:199], v192 offset:1056\n"
" ds_read_b64_tr_b16 a[200:201], v192 offset:64\n"
" ds_read_b64_tr_b16 a[202:203], v192 offset:1088\n"
" buffer_load_dwordx4 a[40:43], v1, s[16:19], s95 offen offset:3072 sc1\n"
" ds_read_b64_tr_b16 a[204:205], v192 offset:96\n"
" ds_read_b64_tr_b16 a[206:207], v192 offset:1120\n"
".LmodB_chk:\n"
" v_xor_b32 v212, s52, v212\n"
" v_xor_b32 v213, s53, v213\n"
" v_xor_b32 v214, s54, v214\n"
" v_xor_b32 v215, s55, v215\n"
" v_xor_b32 v216, s52, v216\n"
" v_xor_b32 v217, s53, v217\n"
" v_xor_b32 v218, s54, v218\n"
" v_xor_b32 v219, s55, v219\n"
" v_xor_b32 v220, s52, v220\n"
" v_xor_b32 v221, s53, v221\n"
" v_xor_b32 v222, s54, v222\n"
" v_xor_b32 v223, s55, v223\n"
" v_xor_b32 v224, s52, v224\n"
" v_xor_b32 v225, s53, v225\n"
" v_xor_b32 v226, s54, v226\n"
" v_xor_b32 v227, s55, v227\n"
" v_xor_b32 v208, s52, v208\n"
" v_xor_b32 v209, s53, v209\n"
" v_xor_b32 v210, s54, v210\n"
" v_xor_b32 v211, s55, v211\n"
" v_or_b32_e64 v10, v212, v213\n"
" v_or3_b32 v10, v10, v214, v215\n"
" v_or3_b32 v10, v10, v216, v217\n"
" v_or3_b32 v10, v10, v218, v219\n"
" v_or3_b32 v10, v10, v220, v221\n"
" v_or3_b32 v10, v10, v222, v223\n"
" v_or3_b32 v10, v10, v224, v225\n"
" v_or3_b32 v10, v10, v226, v227\n"
" v_or3_b32 v10, v10, v208, v209\n"
" v_or3_b32 v10, v10, v210, v211\n"
" v_and_b32 v10, s74, v10\n"
" v_cmp_ne_u32 vcc, 0, v10\n"
" s_nop 0\n"
" s_cbranch_vccnz .LmodB_slow\n"
".LmodB_ok:\n"
" v_pk_add_f32 v[212:213], v[212:213], v[216:217]\n"
" v_pk_add_f32 v[220:221], v[220:221], v[224:225]\n"
" v_pk_add_f32 v[214:215], v[214:215], v[218:219]\n"
" v_pk_add_f32 v[222:223], v[222:223], v[226:227]\n"
" v_pk_add_f32 v[212:213], v[212:213], v[220:221]\n"
" v_pk_add_f32 v[214:215], v[214:215], v[222:223]\n"
" v_permlane32_swap_b32 v208, v209\n"
" v_permlane32_swap_b32 v210, v211\n"
" v_add_f32 v208, v208, v209\n"
" v_add_f32 v210, v210, v211\n"
" s_nop 1\n"
" v_permlane16_swap_b32 v208, v210\n"
" v_add_f32 v208, v208, v210\n"
" v_add_f32 v212, v187, v212\n"
" v_add_f32 v213, v187, v213\n"
" v_add_f32 v214, v187, v214\n"
" v_add_f32 v215, v187, v215\n"
" v_cvt_pk_bf16_f32 v212, v212, v213\n"
" v_cvt_pk_bf16_f32 v213, v214, v215\n"
" ds_write_b64 v9, v[212:213]\n"
" ds_write_b32 v15, v208\n"
" s_waitcnt vmcnt(8)\n"
" s_waitcnt lgkmcnt(0)\n"
" s_barrier\n"
" s_cmp_eq_u32 s3, 3\n"
" s_cbranch_scc0 .LL_f4\n"
" s_lshl_b32 s51, s2, 7\n"
" s_add_u32 s51, s51, 16678912\n"
" v_mov_b32 v232, s48\n"
" v_lshlrev_b32 v233, 2, v0\n"
" s_mov_b32 exec_hi, 0\n"
" buffer_store_dword v232, v233, s[16:19], s51 offen sc1\n"
" s_mov_b64 exec, -1\n"
".LL_f4:\n"
" s_add_u32 s95, s81, 0\n"
" buffer_load_dwordx4 a[8:11], v1, s[16:19], s95 offen offset:0 sc1\n"
" ds_read_b64_tr_b16 v[16:17], v4 offset:0\n"
" ds_read_b64_tr_b16 v[18:19], v4 offset:512\n"
" ds_read_b64_tr_b16 v[20:21], v5 offset:0\n"
" ds_read_b64_tr_b16 v[22:23], v5 offset:512\n"
" ds_read_b64_tr_b16 v[24:25], v6 offset:0\n"
" ds_read_b64_tr_b16 v[26:27], v6 offset:512\n"
" ds_read_b64_tr_b16 v[28:29], v7 offset:0\n"
" buffer_load_dwordx4 a[20:23], v1, s[16:19], s95 offen offset:1024 sc1\n"
" ds_read_b64_tr_b16 v[30:31], v7 offset:512\n"
" ds_read_b64_tr_b16 v[32:33], v4 offset:1024\n"
" ds_read_b64_tr_b16 v[34:35], v4 offset:1536\n"
" ds_read_b64_tr_b16 v[36:37], v5 offset:1024\n"
" ds_read_b64_tr_b16 v[38:39], v5 offset:1536\n"
" ds_read_b64_tr_b16 v[40:41], v6 offset:1024\n"
" ds_read_b64_tr_b16 v[42:43], v6 offset:1536\n"
" buffer_load_dwordx4 a[32:35], v1, s[16:19], s95 offen offset:2048 sc1\n"
" ds_read_b64_tr_b16 v[44:45], v7 offset:1024\n"
" ds_read_b64_tr_b16 v[46:47], v7 offset:1536\n"
" ds_read_b64_tr_b16 v[48:49], v4 offset:2048\n"
" ds_read_b64_tr_b16 v[50:51], v4 offset:2560\n"
" ds_read_b64_tr_b16 v[52:53], v5 offset:2048\n"
" ds_read_b64_tr_b16 v[54:55], v5 offset:2560\n"
" ds_read_b64_tr_b16 v[56:57], v6 offset:2048\n"
" buffer_load_dwordx4 a[44:47], v1, s[16:19], s95 offen offset:3072 sc1\n"
" ds_read_b64_tr_b16 v[58:59], v6 offset:2560\n"
" ds_read_b64_tr_b16 v[60:61], v7 offset:2048\n"
" ds_read_b64_tr_b16 v[62:63], v7 offset:2560\n"
" ds_read_b64_tr_b16 v[64:65], v4 offset:3072\n"
" ds_read_b64_tr_b16 v[66:67], v4 offset:3584\n"
" ds_read_b64_tr_b16 v[68:69], v5 offset:3072\n"
" ds_read_b64_tr_b16 v[70:71], v5 offset:3584\n"
" s_add_u32 s95, s80, 4096\n"
" buffer_load_dwordx4 a[48:51], v1, s[16:19], s95 offen offset:0 sc1\n"
" ds_read_b64_tr_b16 v[72:73], v6 offset:3072\n"
" ds_read_b64_tr_b16 v[74:75], v6 offset:3584\n"
" ds_read_b64_tr_b16 v[76:77], v7 offset:3072\n"
" ds_read_b64_tr_b16 v[78:79], v7 offset:3584\n"
" ds_read_b64_tr_b16 v[80:81], v4 offset:16384\n"
" ds_read_b64_tr_b16 v[82:83], v4 offset:16896\n"
" buffer_load_dwordx4 a[60:63], v1, s[16:19], s95 offen offset:1024 sc1\n"
" ds_read_b64_tr_b16 v[84:85], v5 offset:16384\n"
" ds_read_b64_tr_b16 v[86:87], v5 offset:16896\n"
" ds_read_b64_tr_b16 v[88:89], v6 offset:16384\n"
" ds_read_b64_tr_b16 v[90:91], v6 offset:16896\n"
" ds_read_b64_tr_b16 v[92:93], v7 offset:16384\n"
" ds_read_b64_tr_b16 v[94:95], v7 offset:16896\n"
" ds_read_b128 v[208:211], v13 offset:0\n"
" ds_read_b128 v[212:215], v13 offset:64\n"
" ds_read_b128 v[216:219], v13 offset:128\n"
" ds_read_b128 v[220:223], v13 offset:192\n"
" ds_read_b128 v[224:227], v14 offset:0\n"
" ds_read_b128 v[228:231], v14 offset:64\n"
" s_waitcnt vmcnt(12)\n"
" ds_read_b64 v[96:97], v11\n"
" v_xor_b32 v10, 32, v11\n"
" ds_read_b64 v[98:99], v10\n"
" v_xor_b32 v10, 64, v11\n"
" ds_read_b64 v[100:101], v10\n"
" v_xor_b32 v10, 96, v11\n"
" ds_read_b64 v[102:103], v10\n"
" s_waitcnt lgkmcnt(3)\n"
" v_lshlrev_b32 v192, 16, v96\n"
" v_and_b32 v193, s73, v96\n"
" v_lshlrev_b32 v194, 16, v97\n"
" v_and_b32 v195, s73, v97\n"
" s_waitcnt lgkmcnt(2)\n"
" v_lshlrev_b32 v196, 16, v98\n"
" v_and_b32 v197, s73, v98\n"
" v_lshlrev_b32 v198, 16, v99\n"
" v_and_b32 v199, s73, v99\n"
" s_waitcnt lgkmcnt(1)\n"
" v_lshlrev_b32 v200, 16, v100\n"
" v_and_b32 v201, s73, v100\n"
" v_lshlrev_b32 v202, 16, v101\n"
" v_and_b32 v203, s73, v101\n"
" s_waitcnt lgkmcnt(0)\n"
" v_lshlrev_b32 v204, 16, v102\n"
" v_and_b32 v205, s73, v102\n"
" v_lshlrev_b32 v206, 16, v103\n"
" v_and_b32 v207, s73, v103\n"
" s_sleep 3\n"
" s_mov_b32 s67, 16252928\n"
" s_mov_b32 exec_lo, 0\n"
" s_mov_b32 exec_hi, s61\n"
" buffer_load_dword v237, v238, s[16:19], s67 offen sc1\n"
" s_mov_b64 exec, -1\n"
" s_lshl2_add_u32 s68, s57, s3\n"
" s_lshl_b32 s68, s68, 14\n"
" s_add_u32 s68, s68, 2621440\n"
".Lf4_chk:\n"
" s_waitcnt vmcnt(0)\n"
" v_cmp_ne_u32_e64 vcc, s48, v237\n"
" s_nop 0\n"
" s_cbranch_vccnz .Lf4_slow\n"
".Lf4_ok:\n"
" buffer_load_dwordx4 v[112:115], v1, s[16:19], s68 offen offset:0 sc1\n"
" buffer_load_dwordx4 v[116:119], v1, s[16:19], s68 offen offset:1024 sc1\n"
" buffer_load_dwordx4 v[120:123], v1, s[16:19], s68 offen offset:2048 sc1\n"
" buffer_load_dwordx4 v[124:127], v1, s[16:19], s68 offen offset:3072 sc1\n"
" s_add_u32 s68, s68, 4096\n"
" buffer_load_dwordx4 v[128:131], v1, s[16:19], s68 offen offset:0 sc1\n"
" buffer_load_dwordx4 v[132:135], v1, s[16:19], s68 offen offset:1024 sc1\n"
" buffer_load_dwordx4 v[136:139], v1, s[16:19], s68 offen offset:2048 sc1\n"
" buffer_load_dwordx4 v[140:143], v1, s[16:19], s68 offen offset:3072 sc1\n"
" s_add_u32 s68, s68, 4096\n"
" buffer_load_dwordx4 v[144:147], v1, s[16:19], s68 offen offset:0 sc1\n"
" buffer_load_dwordx4 v[148:151], v1, s[16:19], s68 offen offset:1024 sc1\n"
" s_waitcnt vmcnt(9)\n"
" v_mfma_f32_16x16x32_bf16 v[176:179], v[16:19], v[112:115], 0\n"
" ds_read_b64_tr_b16 v[96:97], v4 offset:17408\n"
" ds_read_b64_tr_b16 v[98:99], v4 offset:17920\n"
" buffer_load_dwordx4 v[152:155], v1, s[16:19], s68 offen offset:2048 sc1\n"
" v_mfma_f32_16x16x32_bf16 v[180:183], v[20:23], v[112:115], 0\n"
" ds_read_b64_tr_b16 v[100:101], v5 offset:17408\n"
" ds_read_b64_tr_b16 v[102:103], v5 offset:17920\n"
" v_mfma_f32_16x16x32_bf16 v[184:187], v[24:27], v[112:115], 0\n"
" ds_read_b64_tr_b16 v[104:105], v6 offset:17408\n"
" v_mfma_f32_16x16x32_bf16 v[188:191], v[28:31], v[112:115], 0\n"
" ds_read_b64_tr_b16 v[106:107], v6 offset:17920\n"
" ds_read_b64_tr_b16 v[108:109], v7 offset:17408\n"
" s_waitcnt vmcnt(9)\n"
" v_mfma_f32_16x16x32_bf16 v[176:179], v[32:35], v[116:119], v[176:179]\n"
" ds_read_b64_tr_b16 v[110:111], v7 offset:17920\n"
" ds_read_b64_tr_b16 v[16:17], v4 offset:18432\n"
" buffer_load_dwordx4 v[156:159], v1, s[16:19], s68 offen offset:3072 sc1\n"
" v_mfma_f32_16x16x32_bf16 v[180:183], v[36:39], v[116:119], v[180:183]\n"
" ds_read_b64_tr_b16 v[18:19], v4 offset:18944\n"
" v_mfma_f32_16x16x32_bf16 v[184:187], v[40:43], v[116:119], v[184:187]\n"
" ds_read_b64_tr_b16 v[20:21], v5 offset:18432\n"
" ds_read_b64_tr_b16 v[22:23], v5 offset:18944\n"
" v_mfma_f32_16x16x32_bf16 v[188:191], v[44:47], v[116:119], v[188:191]\n"
" ds_read_b64_tr_b16 v[24:25], v6 offset:18432\n"
" s_waitcnt vmcnt(9)\n"
" v_mfma_f32_16x16x32_bf16 v[176:179], v[48:51], v[120:123], v[176:179]\n"
" s_add_u32 s68, s68, 4096\n"
" ds_read_b64_tr_b16 v[26:27], v6 offset:18944\n"
" ds_read_b64_tr_b16 v[28:29], v7 offset:18432\n"
" buffer_load_dwordx4 v[160:163], v1, s[16:19], s68 offen offset:0 sc1\n"
" v_mfma_f32_16x16x32_bf16 v[180:183], v[52:55], v[120:123], v[180:183]\n"
" ds_read_b64_tr_b16 v[30:31], v7 offset:18944\n"
" ds_read_b64_tr_b16 v[32:33], v4 offset:19456\n"
" v_mfma_f32_16x16x32_bf16 v[184:187], v[56:59], v[120:123], v[184:187]\n"
" ds_read_b64_tr_b16 v[34:35], v4 offset:19968\n"
" v_mfma_f32_16x16x32_bf16 v[188:191], v[60:63], v[120:123], v[188:191]\n"
" ds_read_b64_tr_b16 v[36:37], v5 offset:19456\n"
" ds_read_b64_tr_b16 v[38:39], v5 offset:19968\n"
" s_waitcnt vmcnt(9)\n"
" v_mfma_f32_16x16x32_bf16 v[176:179], v[64:67], v[124:127], v[176:179]\n"
" ds_read_b64_tr_b16 v[40:41], v6 offset:19456\n"
" buffer_load_dwordx4 v[164:167], v1, s[16:19], s68 offen offset:1024 sc1\n"
" v_mfma_f32_16x16x32_bf16 v[180:183], v[68:71], v[124:127], v[180:183]\n"
" ds_read_b64_tr_b16 v[42:43], v6 offset:19968\n"
" ds_read_b64_tr_b16 v[44:45], v7 offset:19456\n"
" v_mfma_f32_16x16x32_bf16 v[184:187], v[72:75], v[124:127], v[184:187]\n"
" ds_read_b64_tr_b16 v[46:47], v7 offset:19968\n"
" ds_read_b64_tr_b16 v[48:49], v4 offset:32768\n"
" v_mfma_f32_16x16x32_bf16 v[188:191], v[76:79], v[124:127], v[188:191]\n"
" ds_read_b64_tr_b16 v[50:51], v4 offset:33280\n"
" s_waitcnt vmcnt(9)\n"
" v_mfma_f32_16x16x32_bf16 v[176:179], v[80:83], v[128:131], v[176:179]\n"
" ds_read_b64_tr_b16 v[52:53], v5 offset:32768\n"
" ds_read_b64_tr_b16 v[54:55], v5 offset:33280\n"
" buffer_load_dwordx4 v[168:171], v1, s[16:19], s68 offen offset:2048 sc1\n"
" v_mfma_f32_16x16x32_bf16 v[180:183], v[84:87], v[128:131], v[180:183]\n"
" ds_read_b64_tr_b16 v[56:57], v6 offset:32768\n"
" ds_read_b64_tr_b16 v[58:59], v6 offset:33280\n"
" v_mfma_f32_16x16x32_bf16 v[184:187], v[88:91], v[128:131], v[184:187]\n"
" ds_read_b64_tr_b16 v[60:61], v7 offset:32768\n"
" v_mfma_f32_16x16x32_bf16 v[188:191], v[92:95], v[128:131], v[188:191]\n"
" s_waitcnt lgkmcnt(15)\n"
" ds_read_b64_tr_b16 v[62:63], v7 offset:33280\n"
" ds_read_b64_tr_b16 v[64:65], v4 offset:33792\n"
" s_waitcnt vmcnt(9)\n"
" v_mfma_f32_16x16x32_bf16 v[176:179], v[96:99], v[132:135], v[176:179]\n"
" ds_read_b64_tr_b16 v[66:67], v4 offset:34304\n"
" buffer_load_dwordx4 v[172:175], v1, s[16:19], s68 offen offset:3072 sc1\n"
" v_mfma_f32_16x16x32_bf16 v[180:183], v[100:103], v[132:135], v[180:183]\n"
" ds_read_b64_tr_b16 v[68:69], v5 offset:33792\n"
" ds_read_b64_tr_b16 v[70:71], v5 offset:34304\n"
" v_mfma_f32_16x16x32_bf16 v[184:187], v[104:107], v[132:135], v[184:187]\n"
" ds_read_b64_tr_b16 v[72:73], v6 offset:33792\n"
" ds_read_b64_tr_b16 v[74:75], v6 offset:34304\n"
" v_mfma_f32_16x16x32_bf16 v[188:191], v[108:111], v[132:135], v[188:191]\n"
" ds_read_b64_tr_b16 v[76:77], v7 offset:33792\n"
" s_waitcnt vmcnt(9)\n"
" v_mfma_f32_16x16x32_bf16 v[176:179], v[16:19], v[136:139], v[176:179]\n"
" ds_read_b64_tr_b16 v[78:79], v7 offset:34304\n"
" ds_read_b64_tr_b16 v[80:81], v4 offset:34816\n"
" v_mfma_f32_16x16x32_bf16 v[180:183], v[20:23], v[136:139], v[180:183]\n"
" ds_read_b64_tr_b16 v[82:83], v4 offset:35328\n"
" v_mfma_f32_16x16x32_bf16 v[184:187], v[24:27], v[136:139], v[184:187]\n"
" ds_read_b64_tr_b16 v[84:85], v5 offset:34816\n"
" ds_read_b64_tr_b16 v[86:87], v5 offset:35328\n"
" buffer_load_dwordx4 a[72:75], v1, s[16:19], s95 offen offset:2048 sc1\n"
" v_mfma_f32_16x16x32_bf16 v[188:191], v[28:31], v[136:139], v[188:191]\n"
" s_waitcnt lgkmcnt(15)\n"
" ds_read_b64_tr_b16 v[88:89], v6 offset:34816\n"
" ds_read_b64_tr_b16 v[90:91], v6 offset:35328\n"
" s_waitcnt vmcnt(9)\n"
" v_mfma_f32_16x16x32_bf16 v[176:179], v[32:35], v[140:143], v[176:179]\n"
" ds_read_b64_tr_b16 v[92:93], v7 offset:34816\n"
" v_mfma_f32_16x16x32_bf16 v[180:183], v[36:39], v[140:143], v[180:183]\n"
" ds_read_b64_tr_b16 v[94:95], v7 offset:35328\n"
" ds_read_b64_tr_b16 v[96:97], v4 offset:35840\n"
" buffer_load_dwordx4 a[84:87], v1, s[16:19], s95 offen offset:3072 sc1\n"
" v_mfma_f32_16x16x32_bf16 v[184:187], v[40:43], v[140:143], v[184:187]\n"
" ds_read_b64_tr_b16 v[98:99], v4 offset:36352\n"
" v_mfma_f32_16x16x32_bf16 v[188:191], v[44:47], v[140:143], v[188:191]\n"
" s_waitcnt lgkmcnt(15)\n"
" ds_read_b64_tr_b16 v[100:101], v5 offset:35840\n"
" ds_read_b64_tr_b16 v[102:103], v5 offset:36352\n"
" s_waitcnt vmcnt(9)\n"
" v_mfma_f32_16x16x32_bf16 v[176:179], v[48:51], v[144:147], v[176:179]\n"
" s_add_u32 s95, s80, 20480\n"
" ds_read_b64_tr_b16 v[104:105], v6 offset:35840\n"
" ds_read_b64_tr_b16 v[106:107], v6 offset:36352\n"
" buffer_load_dwordx4 a[52:55], v1, s[16:19], s95 offen offset:0 sc1\n"
" v_mfma_f32_16x16x32_bf16 v[180:183], v[52:55], v[144:147], v[180:183]\n"
" ds_read_b64_tr_b16 v[108:109], v7 offset:35840\n"
" v_mfma_f32_16x16x32_bf16 v[184:187], v[56:59], v[144:147], v[184:187]\n"
" ds_read_b64_tr_b16 v[110:111], v7 offset:36352\n"
" ds_read_b64_tr_b16 v[16:17], v4 offset:49152\n"
" v_mfma_f32_16x16x32_bf16 v[188:191], v[60:63], v[144:147], v[188:191]\n"
" s_waitcnt lgkmcnt(15)\n"
" ds_read_b64_tr_b16 v[18:19], v4 offset:49664\n"
" ds_read_b64_tr_b16 v[20:21], v5 offset:49152\n"
" buffer_load_dwordx4 a[64:67], v1, s[16:19], s95 offen offset:1024 sc1\n"
" s_waitcnt vmcnt(10)\n"
" v_mfma_f32_16x16x32_bf16 v[176:179], v[64:67], v[148:151], v[176:179]\n"
" ds_read_b64_tr_b16 v[22:23], v5 offset:49664\n"
" v_mfma_f32_16x16x32_bf16 v[180:183], v[68:71], v[148:151], v[180:183]\n"
" ds_read_b64_tr_b16 v[24:25], v6 offset:49152\n"
" ds_read_b64_tr_b16 v[26:27], v6 offset:49664\n"
" v_mfma_f32_16x16x32_bf16 v[184:187], v[72:75], v[148:151], v[184:187]\n"
" ds_read_b64_tr_b16 v[28:29], v7 offset:49152\n"
" buffer_load_dwordx4 a[76:79], v1, s[16:19], s95 offen offset:2048 sc1\n"
" v_mfma_f32_16x16x32_bf16 v[188:191], v[76:79], v[148:151], v[188:191]\n"
" s_waitcnt lgkmcnt(15)\n"
" ds_read_b64_tr_b16 v[30:31], v7 offset:49664\n"
" ds_read_b64_tr_b16 v[32:33], v4 offset:50176\n"
" s_waitcnt vmcnt(10)\n"
" v_mfma_f32_16x16x32_bf16 v[176:179], v[80:83], v[152:155], v[176:179]\n"
" ds_read_b64_tr_b16 v[34:35], v4 offset:50688\n"
" ds_read_b64_tr_b16 v[36:37], v5 offset:50176\n"
" v_mfma_f32_16x16x32_bf16 v[180:183], v[84:87], v[152:155], v[180:183]\n"
" ds_read_b64_tr_b16 v[38:39], v5 offset:50688\n"
" buffer_load_dwordx4 a[88:91], v1, s[16:19], s95 offen offset:3072 sc1\n"
" v_mfma_f32_16x16x32_bf16 v[184:187], v[88:91], v[152:155], v[184:187]\n"
" ds_read_b64_tr_b16 v[40:41], v6 offset:50176\n"
" ds_read_b64_tr_b16 v[42:43], v6 offset:50688\n"
" v_mfma_f32_16x16x32_bf16 v[188:191], v[92:95], v[152:155], v[188:191]\n"
" s_waitcnt lgkmcnt(14)\n"
" ds_read_b64_tr_b16 v[44:45], v7 offset:50176\n"
" s_waitcnt vmcnt(10)\n"
" v_mfma_f32_16x16x32_bf16 v[176:179], v[96:99], v[156:159], v[176:179]\n"
" s_add_u32 s95, s81, 4096\n"
" ds_read_b64_tr_b16 v[46:47], v7 offset:50688\n"
" ds_read_b64_tr_b16 v[48:49], v4 offset:51200\n"
" buffer_load_dwordx4 a[56:59], v1, s[16:19], s95 offen offset:0 sc1\n"
" v_mfma_f32_16x16x32_bf16 v[180:183], v[100:103], v[156:159], v[180:183]\n"
" ds_read_b64_tr_b16 v[50:51], v4 offset:51712\n"
" ds_read_b64_tr_b16 v[52:53], v5 offset:51200\n"
" v_mfma_f32_16x16x32_bf16 v[184:187], v[104:107], v[156:159], v[184:187]\n"
" ds_read_b64_tr_b16 v[54:55], v5 offset:51712\n"
" v_mfma_f32_16x16x32_bf16 v[188:191], v[108:111], v[156:159], v[188:191]\n"
" s_waitcnt lgkmcnt(12)\n"
" ds_read_b64_tr_b16 v[56:57], v6 offset:51200\n"
" ds_read_b64_tr_b16 v[58:59], v6 offset:51712\n"
" buffer_load_dwordx4 a[68:71], v1, s[16:19], s95 offen offset:1024 sc1\n"
" s_waitcnt vmcnt(11)\n"
" v_mfma_f32_16x16x32_bf16 v[176:179], v[16:19], v[160:163], v[176:179]\n"
" ds_read_b64_tr_b16 v[60:61], v7 offset:51200\n"
" ds_read_b64_tr_b16 v[62:63], v7 offset:51712\n"
" v_mfma_f32_16x16x32_bf16 v[180:183], v[20:23], v[160:163], v[180:183]\n"
" ds_read_b64_tr_b16 v[64:65], v4 offset:52224\n"
" v_mfma_f32_16x16x32_bf16 v[184:187], v[24:27], v[160:163], v[184:187]\n"
" ds_read_b64_tr_b16 v[66:67], v4 offset:52736\n"
" ds_read_b64_tr_b16 v[68:69], v5 offset:52224\n"
" buffer_load_dwordx4 a[80:83], v1, s[16:19], s95 offen offset:2048 sc1\n"
" v_mfma_f32_16x16x32_bf16 v[188:191], v[28:31], v[160:163], v[188:191]\n"
" s_waitcnt lgkmcnt(11)\n"
" ds_read_b64_tr_b16 v[70:71], v5 offset:52736\n"
" s_waitcnt vmcnt(11)\n"
" v_mfma_f32_16x16x32_bf16 v[176:179], v[32:35], v[164:167], v[176:179]\n"
" ds_read_b64_tr_b16 v[72:73], v6 offset:52224\n"
" ds_read_b64_tr_b16 v[74:75], v6 offset:52736\n"
" v_mfma_f32_16x16x32_bf16 v[180:183], v[36:39], v[164:167], v[180:183]\n"
" ds_read_b64_tr_b16 v[76:77], v7 offset:52224\n"
" ds_read_b64_tr_b16 v[78:79], v7 offset:52736\n"
" buffer_load_dwordx4 a[92:95], v1, s[16:19], s95 offen offset:3072 sc1\n"
" v_mfma_f32_16x16x32_bf16 v[184:187], v[40:43], v[164:167], v[184:187]\n"
" v_lshlrev_b32 v16, 16, v224\n"
" v_and_b32 v17, s73, v224\n"
" v_mfma_f32_16x16x32_bf16 v[188:191], v[44:47], v[164:167], v[188:191]\n"
" s_waitcnt lgkmcnt(8)\n"
" v_lshlrev_b32 v18, 16, v225\n"
" v_and_b32 v19, s73, v225\n"
" s_waitcnt vmcnt(11)\n"
" v_mfma_f32_16x16x32_bf16 v[176:179], v[48:51], v[168:171], v[176:179]\n"
" s_add_u32 s95, s80, 8192\n"
" buffer_load_dwordx4 a[96:99], v1, s[16:19], s95 offen offset:0 sc1\n"
" v_lshlrev_b32 v20, 16, v226\n"
" v_mfma_f32_16x16x32_bf16 v[180:183], v[52:55], v[168:171], v[180:183]\n"
" v_and_b32 v21, s73, v226\n"
" v_lshlrev_b32 v22, 16, v227\n"
" v_mfma_f32_16x16x32_bf16 v[184:187], v[56:59], v[168:171], v[184:187]\n"
" v_and_b32 v23, s73, v227\n"
" v_lshlrev_b32 v24, 16, v228\n"
" v_mfma_f32_16x16x32_bf16 v[188:191], v[60:63], v[168:171], v[188:191]\n"
" s_waitcnt lgkmcnt(0)\n"
" buffer_load_dwordx4 a[108:111], v1, s[16:19], s95 offen offset:1024 sc1\n"
" v_and_b32 v25, s73, v228\n"
" s_waitcnt vmcnt(12)\n"
" v_mfma_f32_16x16x32_bf16 v[176:179], v[64:67], v[172:175], v[176:179]\n"
" v_lshlrev_b32 v26, 16, v229\n"
" v_and_b32 v27, s73, v229\n"
" v_mfma_f32_16x16x32_bf16 v[180:183], v[68:71], v[172:175], v[180:183]\n"
" v_lshlrev_b32 v28, 16, v230\n"
" v_and_b32 v29, s73, v230\n"
" v_mfma_f32_16x16x32_bf16 v[184:187], v[72:75], v[172:175], v[184:187]\n"
" v_lshlrev_b32 v30, 16, v231\n"
" v_and_b32 v31, s73, v231\n"
" v_mfma_f32_16x16x32_bf16 v[188:191], v[76:79], v[172:175], v[188:191]\n"
" v_pk_fma_f32 v[192:193], v[176:177], v[208:209], v[192:193]\n"
" v_pk_fma_f32 v[194:195], v[178:179], v[210:211], v[194:195]\n"
" v_pk_fma_f32 v[196:197], v[180:181], v[212:213], v[196:197]\n"
" v_pk_fma_f32 v[198:199], v[182:183], v[214:215], v[198:199]\n"
" s_nop 0\n"
" v_pk_fma_f32 v[200:201], v[184:185], v[216:217], v[200:201]\n"
" v_pk_fma_f32 v[202:203], v[186:187], v[218:219], v[202:203]\n"
" s_nop 0\n"
" v_pk_fma_f32 v[204:205], v[188:189], v[220:221], v[204:205]\n"
" v_pk_fma_f32 v[206:207], v[190:191], v[222:223], v[206:207]\n"
" v_pk_mul_f32 v[176:177], v[192:193], v[16:17]\n"
" v_pk_mul_f32 v[178:179], v[194:195], v[18:19]\n"
" v_pk_mul_f32 v[180:181], v[196:197], v[20:21]\n"
" v_pk_mul_f32 v[182:183], v[198:199], v[22:23]\n"
" v_pk_mul_f32 v[184:185], v[200:201], v[24:25]\n"
" v_pk_mul_f32 v[186:187], v[202:203], v[26:27]\n"
" v_pk_mul_f32 v[188:189], v[204:205], v[28:29]\n"
" v_pk_mul_f32 v[190:191], v[206:207], v[30:31]\n"
" v_cvt_pk_bf16_f32 v208, v176, v177\n"
" v_cvt_pk_bf16_f32 v209, v178, v179\n"
" v_cvt_pk_bf16_f32 v210, v180, v181\n"
" v_cvt_pk_bf16_f32 v211, v182, v183\n"
" v_cvt_pk_bf16_f32 v212, v184, v185\n"
" v_cvt_pk_bf16_f32 v213, v186, v187\n"
" v_cvt_pk_bf16_f32 v214, v188, v189\n"
" v_cvt_pk_bf16_f32 v215, v190, v191\n"
" buffer_store_dwordx4 v[208:211], v1, s[16:19], s92 offen offset:0 sc1\n"
" buffer_store_dwordx4 v[212:215], v1, s[16:19], s92 offen offset:1024 sc1\n"
" v_pk_add_f32 v[176:177], v[192:193], v[194:195]\n"
" v_pk_mul_f32 v[178:179], v[192:193], v[192:193]\n"
" v_pk_fma_f32 v[178:179], v[194:195], v[194:195], v[178:179]\n"
" v_pk_add_f32 v[176:177], v[176:177], v[196:197]\n"
" v_pk_fma_f32 v[178:179], v[196:197], v[196:197], v[178:179]\n"
" v_pk_add_f32 v[176:177], v[176:177], v[198:199]\n"
" v_pk_fma_f32 v[178:179], v[198:199], v[198:199], v[178:179]\n"
" v_pk_add_f32 v[176:177], v[176:177], v[200:201]\n"
" v_pk_fma_f32 v[178:179], v[200:201], v[200:201], v[178:179]\n"
" v_pk_add_f32 v[176:177], v[176:177], v[202:203]\n"
" v_pk_fma_f32 v[178:179], v[202:203], v[202:203], v[178:179]\n"
" v_pk_add_f32 v[176:177], v[176:177], v[204:205]\n"
" v_pk_fma_f32 v[178:179], v[204:205], v[204:205], v[178:179]\n"
" v_pk_add_f32 v[176:177], v[176:177], v[206:207]\n"
" v_pk_fma_f32 v[178:179], v[206:207], v[206:207], v[178:179]\n"
" v_add_f32 v176, v176, v177\n"
" v_add_f32 v178, v178, v179\n"
" s_nop 1\n"
" v_permlane32_swap_b32 v176, v178\n"
" v_add_f32 v176, v176, v178\n"
" v_mov_b32 v177, v176\n"
" s_nop 1\n"
" v_permlane16_swap_b32 v176, v177\n"
" v_add_f32 v176, v176, v177\n"
" buffer_store_dword v176, v240, s[16:19], s93 offen sc1\n"
" buffer_load_dwordx4 a[120:123], v1, s[16:19], s95 offen offset:2048 sc1\n"
" v_lshlrev_b32 v4, 4, v2\n"
" v_xor_b32 v4, v4, v1\n"
" v_add_u32 v12, s72, v4\n"
" v_xor_b32 v13, 64, v12\n"
" v_xor_b32 v14, 128, v12\n"
" v_xor_b32 v15, 192, v12\n"
" ds_write_b128 v12, v[192:195] offset:0\n"
" buffer_load_dwordx4 a[132:135], v1, s[16:19], s95 offen offset:3072 sc1\n"
" ds_write_b128 v13, v[196:199] offset:1024\n"
" s_add_u32 s95, s80, 24576\n"
" buffer_load_dwordx4 a[100:103], v1, s[16:19], s95 offen offset:0 sc1\n"
" buffer_load_dwordx4 a[112:115], v1, s[16:19], s95 offen offset:1024 sc1\n"
" ds_write_b128 v14, v[200:203] offset:2048\n"
" buffer_load_dwordx4 a[124:127], v1, s[16:19], s95 offen offset:2048 sc1\n"
" ds_write_b128 v15, v[204:207] offset:3072\n"
" buffer_load_dwordx4 a[136:139], v1, s[16:19], s95 offen offset:3072 sc1\n"
" v_bfe_u32 v10, v0, 3, 1\n"
" v_lshlrev_b32 v10, 10, v10\n"
" v_lshl_add_u32 v10, v2, 4, v10\n"
" v_add_u32 v10, 158208, v10\n"
" ds_read_b128 v[144:147], v10 offset:0\n"
" s_add_u32 s95, s81, 8192\n"
" buffer_load_dwordx4 a[104:107], v1, s[16:19], s95 offen offset:0 sc1\n"
" ds_read_b128 v[148:151], v10 offset:64\n"
" ds_read_b128 v[152:155], v10 offset:128\n"
" ds_read_b128 v[156:159], v10 offset:192\n"
" buffer_load_dwordx4 a[116:119], v1, s[16:19], s95 offen offset:1024 sc1\n"
" ds_read_b128 v[160:163], v10 offset:256\n"
" ds_read_b128 v[164:167], v10 offset:320\n"
" ds_read_b128 v[168:171], v10 offset:384\n"
" buffer_load_dwordx4 a[128:131], v1, s[16:19], s95 offen offset:2048 sc1\n"
" ds_read_b128 v[172:175], v10 offset:448\n"
" ds_read_b128 v[176:179], v10 offset:512\n"
" ds_read_b128 v[180:183], v10 offset:576\n"
" buffer_load_dwordx4 a[140:143], v1, s[16:19], s95 offen offset:3072 sc1\n"
" ds_read_b128 v[184:187], v10 offset:640\n"
" ds_read_b128 v[188:191], v10 offset:704\n"
" ds_read_b128 v[192:195], v10 offset:768\n"
" ds_read_b128 v[196:199], v10 offset:832\n"
" ds_read_b128 v[200:203], v10 offset:896\n"
" ds_read_b128 v[204:207], v10 offset:960\n"
" v_add_u32 v8, 65536, v1\n"
" v_lshlrev_b32 v11, 4, v3\n"
" v_add_u32 v11, 160768, v11\n"
" v_and_b32 v6, 1, v2\n"
" v_lshrrev_b32 v7, 1, v2\n"
" v_lshl_add_u32 v6, v6, 1, v7\n"
" v_lshl_add_u32 v6, v3, 2, v6\n"
" s_lshl_b32 s65, s3, 8\n"
" s_add_u32 s65, s65, 160768\n"
" v_lshl_add_u32 v7, v6, 4, s65\n"
" s_mul_i32 s65, s58, 40\n"
" s_add_u32 s65, s65, s3\n"
" s_lshl_b32 s65, s65, 12\n"
" s_add_u32 s88, s65, 9633792\n"
" s_cmp_eq_u32 s3, 3\n"
" s_cselect_b32 s89, 1073741824, 0\n"
" s_and_b32 s66, s57, 1\n"
" s_lshl3_add_u32 s65, s62, s66\n"
" s_mul_i32 s65, s65, 43\n"
" s_add_u32 s65, s65, s3\n"
" s_lshl_b32 s65, s65, 10\n"
" s_add_u32 s90, s65, 11042816\n"
" v_lshrrev_b32 v6, 5, v0\n"
" v_mul_u32_u24 v6, 43520, v6\n"
" v_add_u32 v9, v6, v1\n"
" s_lshl3_add_u32 s86, s62, s3\n"
" s_mov_b32 s52, 0xffff\n"
" s_mov_b32 s53, 0xffff\n"
" s_waitcnt vmcnt(10)\n"
" s_waitcnt lgkmcnt(0)\n"
" s_barrier\n"
" s_cmp_eq_u32 s3, 3\n"
" s_cbranch_scc0 .LL_f2\n"
" s_lshl_b32 s51, s2, 7\n"
" s_add_u32 s51, s51, 458752\n"
" v_mov_b32 v232, s48\n"
" v_lshlrev_b32 v233, 2, v0\n"
" s_mov_b32 exec_hi, 0\n"
" buffer_store_dword v232, v233, s[16:19], s51 offen sc1\n"
" s_mov_b64 exec, -1\n"
".LL_f2:\n"
" s_add_u32 s95, s80, 12288\n"
" buffer_load_dwordx4 a[144:147], v1, s[16:19], s95 offen offset:0 sc1\n"
" v_mfma_f32_16x16x32_bf16 v[220:223], a[0:3], v[144:147], 0\n"
" v_mfma_f32_16x16x32_bf16 v[224:227], a[4:7], v[144:147], 0\n"
" v_mfma_f32_16x16x32_bf16 v[228:231], a[8:11], v[144:147], 0\n"
" v_mfma_f32_16x16x32_bf16 v[220:223], a[12:15], v[148:151], v[220:223]\n"
" buffer_load_dwordx4 a[156:159], v1, s[16:19], s95 offen offset:1024 sc1\n"
" v_mfma_f32_16x16x32_bf16 v[224:227], a[16:19], v[148:151], v[224:227]\n"
" v_mfma_f32_16x16x32_bf16 v[228:231], a[20:23], v[148:151], v[228:231]\n"
" v_mfma_f32_16x16x32_bf16 v[220:223], a[24:27], v[152:155], v[220:223]\n"
" v_mfma_f32_16x16x32_bf16 v[224:227], a[28:31], v[152:155], v[224:227]\n"
" buffer_load_dwordx4 a[168:171], v1, s[16:19], s95 offen offset:2048 sc1\n"
" v_mfma_f32_16x16x32_bf16 v[228:231], a[32:35], v[152:155], v[228:231]\n"
" v_mfma_f32_16x16x32_bf16 v[220:223], a[36:39], v[156:159], v[220:223]\n"
" v_mfma_f32_16x16x32_bf16 v[224:227], a[40:43], v[156:159], v[224:227]\n"
" v_mfma_f32_16x16x32_bf16 v[228:231], a[44:47], v[156:159], v[228:231]\n"
" buffer_load_dwordx4 a[180:183], v1, s[16:19], s95 offen offset:3072 sc1\n"
" v_mfma_f32_16x16x32_bf16 v[220:223], a[48:51], v[160:163], v[220:223]\n"
" v_mfma_f32_16x16x32_bf16 v[224:227], a[52:55], v[160:163], v[224:227]\n"
" v_mfma_f32_16x16x32_bf16 v[228:231], a[56:59], v[160:163], v[228:231]\n"
" v_mfma_f32_16x16x32_bf16 v[220:223], a[60:63], v[164:167], v[220:223]\n"
" s_add_u32 s95, s80, 28672\n"
" buffer_load_dwordx4 a[148:151], v1, s[16:19], s95 offen offset:0 sc1\n"
" v_mfma_f32_16x16x32_bf16 v[224:227], a[64:67], v[164:167], v[224:227]\n"
" v_mfma_f32_16x16x32_bf16 v[228:231], a[68:71], v[164:167], v[228:231]\n"
" v_mfma_f32_16x16x32_bf16 v[220:223], a[72:75], v[168:171], v[220:223]\n"
" v_mfma_f32_16x16x32_bf16 v[224:227], a[76:79], v[168:171], v[224:227]\n"
" buffer_load_dwordx4 a[160:163], v1, s[16:19], s95 offen offset:1024 sc1\n"
" v_mfma_f32_16x16x32_bf16 v[228:231], a[80:83], v[168:171], v[228:231]\n"
" v_mfma_f32_16x16x32_bf16 v[220:223], a[84:87], v[172:175], v[220:223]\n"
" v_mfma_f32_16x16x32_bf16 v[224:227], a[88:91], v[172:175], v[224:227]\n"
" v_mfma_f32_16x16x32_bf16 v[228:231], a[92:95], v[172:175], v[228:231]\n"
" buffer_load_dwordx4 a[172:175], v1, s[16:19], s95 offen offset:2048 sc1\n"
" v_mfma_f32_16x16x32_bf16 v[220:223], a[96:99], v[176:179], v[220:223]\n"
" s_waitcnt vmcnt(14)\n"
" v_mfma_f32_16x16x32_bf16 v[224:227], a[100:103], v[176:179], v[224:227]\n"
" s_waitcnt vmcnt(10)\n"
" v_mfma_f32_16x16x32_bf16 v[228:231], a[104:107], v[176:179], v[228:231]\n"
" v_mfma_f32_16x16x32_bf16 v[220:223], a[108:111], v[180:183], v[220:223]\n"
" buffer_load_dwordx4 a[184:187], v1, s[16:19], s95 offen offset:3072 sc1\n"
" v_mfma_f32_16x16x32_bf16 v[224:227], a[112:115], v[180:183], v[224:227]\n"
" s_waitcnt vmcnt(10)\n"
" v_mfma_f32_16x16x32_bf16 v[228:231], a[116:119], v[180:183], v[228:231]\n"
" v_mfma_f32_16x16x32_bf16 v[220:223], a[120:123], v[184:187], v[220:223]\n"
" v_mfma_f32_16x16x32_bf16 v[224:227], a[124:127], v[184:187], v[224:227]\n"
" s_add_u32 s95, s81, 12288\n"
" buffer_load_dwordx4 a[152:155], v1, s[16:19], s95 offen offset:0 sc1\n"
" s_waitcnt vmcnt(10)\n"
" v_mfma_f32_16x16x32_bf16 v[228:231], a[128:131], v[184:187], v[228:231]\n"
" v_mfma_f32_16x16x32_bf16 v[220:223], a[132:135], v[188:191], v[220:223]\n"
" v_mfma_f32_16x16x32_bf16 v[224:227], a[136:139], v[188:191], v[224:227]\n"
" s_waitcnt vmcnt(9)\n"
" v_mfma_f32_16x16x32_bf16 v[228:231], a[140:143], v[188:191], v[228:231]\n"
" buffer_load_dwordx4 a[164:167], v1, s[16:19], s95 offen offset:1024 sc1\n"
" buffer_load_dwordx4 a[176:179], v1, s[16:19], s95 offen offset:2048 sc1\n"
" buffer_load_dwordx4 a[188:191], v1, s[16:19], s95 offen offset:3072 sc1\n"
" s_mov_b32 s67, 32768\n"
" s_mov_b32 exec_lo, 0\n"
" s_mov_b32 exec_hi, 0xffff0000\n"
" buffer_load_dword v237, v238, s[16:19], s67 offen sc1\n"
" s_mov_b64 exec, -1\n"
" s_add_u32 s95, s88, 0\n"
" buffer_load_dwordx4 a[208:211], v1, s[16:19], s95 offen offset:0 sc1\n"
" buffer_load_dwordx4 a[212:215], v1, s[16:19], s95 offen offset:1024 sc1\n"
" buffer_load_dwordx4 a[216:219], v1, s[16:19], s95 offen offset:2048 sc1\n"
" buffer_load_dwordx4 a[220:223], v1, s[16:19], s95 offen offset:3072 sc1\n"
" s_waitcnt vmcnt(16)\n"
" v_mfma_f32_16x16x32_bf16 v[220:223], a[144:147], v[192:195], v[220:223]\n"
" s_waitcnt vmcnt(12)\n"
" v_mfma_f32_16x16x32_bf16 v[224:227], a[148:151], v[192:195], v[224:227]\n"
" s_waitcnt vmcnt(8)\n"
" v_mfma_f32_16x16x32_bf16 v[228:231], a[152:155], v[192:195], v[228:231]\n"
" v_mfma_f32_16x16x32_bf16 v[220:223], a[156:159], v[196:199], v[220:223]\n"
" v_mfma_f32_16x16x32_bf16 v[224:227], a[160:163], v[196:199], v[224:227]\n"
" s_waitcnt vmcnt(7)\n"
" v_mfma_f32_16x16x32_bf16 v[228:231], a[164:167], v[196:199], v[228:231]\n"
" v_mfma_f32_16x16x32_bf16 v[220:223], a[168:171], v[200:203], v[220:223]\n"
" v_mfma_f32_16x16x32_bf16 v[224:227], a[172:175], v[200:203], v[224:227]\n"
" s_waitcnt vmcnt(6)\n"
" v_mfma_f32_16x16x32_bf16 v[228:231], a[176:179], v[200:203], v[228:231]\n"
" v_mfma_f32_16x16x32_bf16 v[220:223], a[180:183], v[204:207], v[220:223]\n"
" v_mfma_f32_16x16x32_bf16 v[224:227], a[184:187], v[204:207], v[224:227]\n"
" s_waitcnt vmcnt(5)\n"
" v_mfma_f32_16x16x32_bf16 v[228:231], a[188:191], v[204:207], v[228:231]\n"
" s_nop 4\n"
" v_mov_b32 v208, v220\n"
" v_mov_b32 v209, v221\n"
" v_mov_b32 v210, v222\n"
" v_mov_b32 v211, v223\n"
" v_mov_b32 v212, v224\n"
" v_mov_b32 v213, v225\n"
" v_mov_b32 v214, v226\n"
" v_mov_b32 v215, v227\n"
" v_mov_b32 v216, v228\n"
" v_mov_b32 v217, v229\n"
" v_mov_b32 v218, v230\n"
" v_mov_b32 v219, v231\n"
" v_mov_b32_dpp v208, v220 row_ror:8 row_mask:0xf bank_mask:0x3\n"
" v_mov_b32_dpp v209, v221 row_ror:8 row_mask:0xf bank_mask:0x3\n"
" v_mov_b32_dpp v210, v222 row_ror:8 row_mask:0xf bank_mask:0x3\n"
" v_mov_b32_dpp v211, v223 row_ror:8 row_mask:0xf bank_mask:0x3\n"
" v_mov_b32_dpp v212, v224 row_ror:8 row_mask:0xf bank_mask:0x3\n"
" v_mov_b32_dpp v213, v225 row_ror:8 row_mask:0xf bank_mask:0x3\n"
" v_mov_b32_dpp v214, v226 row_ror:8 row_mask:0xf bank_mask:0x3\n"
" v_mov_b32_dpp v215, v227 row_ror:8 row_mask:0xf bank_mask:0x3\n"
" v_mov_b32_dpp v216, v228 row_ror:8 row_mask:0xf bank_mask:0x3\n"
" v_mov_b32_dpp v217, v229 row_ror:8 row_mask:0xf bank_mask:0x3\n"
" v_mov_b32_dpp v218, v230 row_ror:8 row_mask:0xf bank_mask:0x3\n"
" v_mov_b32_dpp v219, v231 row_ror:8 row_mask:0xf bank_mask:0x3\n"
" v_mov_b32_dpp v220, v220 row_ror:8 row_mask:0xf bank_mask:0xc\n"
" v_mov_b32_dpp v221, v221 row_ror:8 row_mask:0xf bank_mask:0xc\n"
" v_mov_b32_dpp v222, v222 row_ror:8 row_mask:0xf bank_mask:0xc\n"
" v_mov_b32_dpp v223, v223 row_ror:8 row_mask:0xf bank_mask:0xc\n"
" v_mov_b32_dpp v224, v224 row_ror:8 row_mask:0xf bank_mask:0xc\n"
" v_mov_b32_dpp v225, v225 row_ror:8 row_mask:0xf bank_mask:0xc\n"
" v_mov_b32_dpp v226, v226 row_ror:8 row_mask:0xf bank_mask:0xc\n"
" v_mov_b32_dpp v227, v227 row_ror:8 row_mask:0xf bank_mask:0xc\n"
" v_mov_b32_dpp v228, v228 row_ror:8 row_mask:0xf bank_mask:0xc\n"
" v_mov_b32_dpp v229, v229 row_ror:8 row_mask:0xf bank_mask:0xc\n"
" v_mov_b32_dpp v230, v230 row_ror:8 row_mask:0xf bank_mask:0xc\n"
" v_mov_b32_dpp v231, v231 row_ror:8 row_mask:0xf bank_mask:0xc\n"
" s_lshl_b32 s69, s62, 5\n"
" s_add_u32 s69, s69, s3\n"
" s_lshl_b32 s69, s69, 12\n"
" s_add_u32 s69, s69, 4718592\n"
" s_lshl_b32 m0, s3, 12\n"
".Lf2_chk:\n"
" s_waitcnt vmcnt(4)\n"
" v_cmp_ne_u32 vcc, s48, v237\n"
" s_nop 0\n"
" s_cbranch_vccnz .Lf2_slow\n"
".Lf2_ok:\n"
" buffer_load_dwordx4 v1, s[16:19], s69 offen offset:0 sc1 lds\n"
" buffer_load_dwordx4 v1, s[16:19], s69 offen offset:1024 sc1 lds\n"
" buffer_load_dwordx4 v1, s[16:19], s69 offen offset:2048 sc1 lds\n"
" buffer_load_dwordx4 v1, s[16:19], s69 offen offset:3072 sc1 lds\n"
" s_add_u32 s69, s69, 16384\n"
" s_add_u32 m0, m0, 16384\n"
" s_lshl_b32 s67, s86, 10\n"
" s_nop 0\n"
" s_add_u32 s67, s67, 294912\n"
" s_add_u32 s68, s67, 4096\n"
" buffer_load_dwordx4 v[72:75], v1, s[16:19], s67 offen sc1\n"
" buffer_load_dwordx4 v[76:79], v1, s[16:19], s68 offen sc1\n"
" buffer_load_dwordx4 v1, s[16:19], s69 offen offset:0 sc1 lds\n"
" buffer_load_dwordx4 v1, s[16:19], s69 offen offset:1024 sc1 lds\n"
" buffer_load_dwordx4 v1, s[16:19], s69 offen offset:2048 sc1 lds\n"
" buffer_load_dwordx4 v1, s[16:19], s69 offen offset:3072 sc1 lds\n"
" s_add_u32 s69, s69, 16384\n"
" s_add_u32 m0, m0, 16384\n"
" s_waitcnt vmcnt(6)\n"
" s_barrier\n"
" ds_read_b128 v[16:19], v1 offset:0\n"
" ds_read_b128 v[20:23], v1 offset:1024\n"
" ds_read_b128 v[24:27], v1 offset:2048\n"
" ds_read_b128 v[28:31], v1 offset:3072\n"
" ds_read_b128 v[32:35], v1 offset:4096\n"
" s_waitcnt lgkmcnt(3)\n"
" v_mfma_f32_16x16x32_bf16 v[48:51], a[0:3], v[16:19], 0\n"
" ds_read_b128 v[36:39], v1 offset:5120\n"
" v_mfma_f32_16x16x32_bf16 v[52:55], a[4:7], v[16:19], 0\n"
" buffer_load_dwordx4 v1, s[16:19], s69 offen offset:0 sc1 lds\n"
" v_mfma_f32_16x16x32_bf16 v[56:59], a[8:11], v[16:19], 0\n"
" v_mfma_f32_16x16x32_bf16 v[48:51], a[12:15], v[20:23], v[48:51]\n"
" ds_read_b128 v[40:43], v1 offset:6144\n"
" v_mfma_f32_16x16x32_bf16 v[52:55], a[16:19], v[20:23], v[52:55]\n"
" v_mfma_f32_16x16x32_bf16 v[56:59], a[20:23], v[20:23], v[56:59]\n"
" s_waitcnt lgkmcnt(3)\n"
" buffer_load_dwordx4 v1, s[16:19], s69 offen offset:1024 sc1 lds\n"
" v_mfma_f32_16x16x32_bf16 v[48:51], a[24:27], v[24:27], v[48:51]\n"
" ds_read_b128 v[44:47], v1 offset:7168\n"
" v_mfma_f32_16x16x32_bf16 v[52:55], a[28:31], v[24:27], v[52:55]\n"
" v_mfma_f32_16x16x32_bf16 v[56:59], a[32:35], v[24:27], v[56:59]\n"
" s_waitcnt vmcnt(7)\n"
" v_permlane32_swap_b32 v72, v73\n"
" v_mfma_f32_16x16x32_bf16 v[48:51], a[36:39], v[28:31], v[48:51]\n"
" ds_read_b128 v[16:19], v1 offset:8192\n"
" v_mfma_f32_16x16x32_bf16 v[52:55], a[40:43], v[28:31], v[52:55]\n"
" buffer_load_dwordx4 v1, s[16:19], s69 offen offset:2048 sc1 lds\n"
" v_mfma_f32_16x16x32_bf16 v[56:59], a[44:47], v[28:31], v[56:59]\n"
" s_waitcnt lgkmcnt(3)\n"
" s_waitcnt vmcnt(7)\n"
" v_permlane32_swap_b32 v76, v77\n"
" v_mfma_f32_16x16x32_bf16 v[48:51], a[48:51], v[32:35], v[48:51]\n"
" ds_read_b128 v[20:23], v1 offset:9216\n"
" v_mfma_f32_16x16x32_bf16 v[52:55], a[52:55], v[32:35], v[52:55]\n"
" v_permlane32_swap_b32 v74, v75\n"
" v_mfma_f32_16x16x32_bf16 v[56:59], a[56:59], v[32:35], v[56:59]\n"
" v_permlane32_swap_b32 v78, v79\n"
" v_mfma_f32_16x16x32_bf16 v[48:51], a[60:63], v[36:39], v[48:51]\n"
" ds_read_b128 v[24:27], v1 offset:10240\n"
" v_add_f32 v72, v72, v73\n"
" v_mfma_f32_16x16x32_bf16 v[52:55], a[64:67], v[36:39], v[52:55]\n"
" buffer_load_dwordx4 v1, s[16:19], s69 offen offset:3072 sc1 lds\n"
" s_add_u32 s69, s69, 16384\n"
" s_add_u32 m0, m0, 16384\n"
" v_mfma_f32_16x16x32_bf16 v[56:59], a[68:71], v[36:39], v[56:59]\n"
" s_waitcnt lgkmcnt(3)\n"
" v_add_f32 v76, v76, v77\n"
" v_add_f32 v74, v74, v75\n"
" v_mfma_f32_16x16x32_bf16 v[48:51], a[72:75], v[40:43], v[48:51]\n"
" ds_read_b128 v[28:31], v1 offset:11264\n"
" v_add_f32 v78, v78, v79\n"
" v_mfma_f32_16x16x32_bf16 v[52:55], a[76:79], v[40:43], v[52:55]\n"
" v_permlane16_swap_b32 v72, v74\n"
" v_mfma_f32_16x16x32_bf16 v[56:59], a[80:83], v[40:43], v[56:59]\n"
" buffer_load_dwordx4 v1, s[16:19], s69 offen offset:0 sc1 lds\n"
" v_mfma_f32_16x16x32_bf16 v[48:51], a[84:87], v[44:47], v[48:51]\n"
" ds_read_b128 v[32:35], v1 offset:12288\n"
" v_mfma_f32_16x16x32_bf16 v[52:55], a[88:91], v[44:47], v[52:55]\n"
" v_permlane16_swap_b32 v76, v78\n"
" v_mfma_f32_16x16x32_bf16 v[56:59], a[92:95], v[44:47], v[56:59]\n"
" s_waitcnt lgkmcnt(3)\n"
" v_add_f32 v72, v72, v74\n"
" v_add_f32 v76, v76, v78\n"
" v_mfma_f32_16x16x32_bf16 v[48:51], a[96:99], v[16:19], v[48:51]\n"
" ds_read_b128 v[36:39], v1 offset:13312\n"
" v_add_f32_dpp v72, v72, v72 row_ror:8 row_mask:0xf bank_mask:0xf\n"
" v_mfma_f32_16x16x32_bf16 v[52:55], a[100:103], v[16:19], v[52:55]\n"
" buffer_load_dwordx4 v1, s[16:19], s69 offen offset:1024 sc1 lds\n"
" v_add_f32_dpp v76, v76, v76 row_ror:8 row_mask:0xf bank_mask:0xf\n"
" v_mfma_f32_16x16x32_bf16 v[56:59], a[104:107], v[16:19], v[56:59]\n"
" v_mul_f32 v72, 0x3b000000, v72\n"
" v_mul_f32 v76, 0x3b000000, v76\n"
" v_mfma_f32_16x16x32_bf16 v[48:51], a[108:111], v[20:23], v[48:51]\n"
" ds_read_b128 v[40:43], v1 offset:14336\n"
" v_mov_b32_dpp v73, v72 row_ror:4 row_mask:0xf bank_mask:0xf\n"
" v_mfma_f32_16x16x32_bf16 v[52:55], a[112:115], v[20:23], v[52:55]\n"
" v_mov_b32_dpp v77, v76 row_ror:4 row_mask:0xf bank_mask:0xf\n"
" v_fma_f32 v73, -v72, v72, v73\n"
" v_mfma_f32_16x16x32_bf16 v[56:59], a[116:119], v[20:23], v[56:59]\n"
" s_waitcnt lgkmcnt(3)\n"
" buffer_load_dwordx4 v1, s[16:19], s69 offen offset:2048 sc1 lds\n"
" v_fma_f32 v77, -v76, v76, v77\n"
" v_mfma_f32_16x16x32_bf16 v[48:51], a[120:123], v[24:27], v[48:51]\n"
" ds_read_b128 v[44:47], v1 offset:15360\n"
" v_max_f32 v73, 0, v73\n"
" v_mfma_f32_16x16x32_bf16 v[52:55], a[124:127], v[24:27], v[52:55]\n"
" v_max_f32 v77, 0, v77\n"
" v_add_f32 v73, 0x3727c5ac, v73\n"
" v_mfma_f32_16x16x32_bf16 v[56:59], a[128:131], v[24:27], v[56:59]\n"
" s_waitcnt vmcnt(7)\n"
" v_add_f32 v77, 0x3727c5ac, v77\n"
" s_barrier\n"
" v_mfma_f32_16x16x32_bf16 v[48:51], a[132:135], v[28:31], v[48:51]\n"
" ds_read_b128 v[16:19], v1 offset:16384\n"
" v_sub_f32 v80, 0, v72\n"
" v_mfma_f32_16x16x32_bf16 v[52:55], a[136:139], v[28:31], v[52:55]\n"
" buffer_load_dwordx4 v1, s[16:19], s69 offen offset:3072 sc1 lds\n"
" s_add_u32 s69, s69, 16384\n"
" s_add_u32 m0, m0, 16384\n"
" v_mfma_f32_16x16x32_bf16 v[56:59], a[140:143], v[28:31], v[56:59]\n"
" s_waitcnt lgkmcnt(3)\n"
" v_sub_f32 v84, 0, v76\n"
" v_mfma_f32_16x16x32_bf16 v[48:51], a[144:147], v[32:35], v[48:51]\n"
" ds_read_b128 v[20:23], v1 offset:17408\n"
" v_mfma_f32_16x16x32_bf16 v[52:55], a[148:151], v[32:35], v[52:55]\n"
" v_sqrt_f32 v81, v73\n"
" v_mfma_f32_16x16x32_bf16 v[56:59], a[152:155], v[32:35], v[56:59]\n"
" v_sqrt_f32 v85, v77\n"
" v_mfma_f32_16x16x32_bf16 v[48:51], a[156:159], v[36:39], v[48:51]\n"
" ds_read_b128 v[24:27], v1 offset:18432\n"
" v_mfma_f32_16x16x32_bf16 v[52:55], a[160:163], v[36:39], v[52:55]\n"
" buffer_load_dwordx4 v1, s[16:19], s69 offen offset:0 sc1 lds\n"
" v_mfma_f32_16x16x32_bf16 v[56:59], a[164:167], v[36:39], v[56:59]\n"
" s_waitcnt lgkmcnt(3)\n"
" v_rcp_f32 v82, v73\n"
" v_mfma_f32_16x16x32_bf16 v[48:51], a[168:171], v[40:43], v[48:51]\n"
" ds_read_b128 v[28:31], v1 offset:19456\n"
" v_mfma_f32_16x16x32_bf16 v[52:55], a[172:175], v[40:43], v[52:55]\n"
" v_rcp_f32 v86, v77\n"
" s_mov_b32 exec_lo, 0x000f000f\n"
" s_mov_b32 exec_hi, 0x000f000f\n"
" ds_write_b128 v7, v[80:83] offset:0\n"
" ds_write_b128 v7, v[84:87] offset:1024\n"
" s_mov_b64 exec, -1\n"
" s_nop 1\n"
" v_mfma_f32_16x16x32_bf16 v[56:59], a[176:179], v[40:43], v[56:59]\n"
" s_waitcnt lgkmcnt(0)\n"
" s_barrier\n"
" buffer_load_dwordx4 v1, s[16:19], s69 offen offset:1024 sc1 lds\n"
" v_mfma_f32_16x16x32_bf16 v[48:51], a[180:183], v[44:47], v[48:51]\n"
" ds_read_b128 v[32:35], v1 offset:20480\n"
" v_mfma_f32_16x16x32_bf16 v[52:55], a[184:187], v[44:47], v[52:55]\n"
" ds_read_b128 v[96:99], v11 offset:0\n"
" v_mfma_f32_16x16x32_bf16 v[56:59], a[188:191], v[44:47], v[56:59]\n"
" ds_read_b128 v[100:103], v11 offset:256\n"
" ds_read_b128 v[104:107], v11 offset:1536\n"
" ds_read_b128 v[108:111], v11 offset:1792\n"
" v_mfma_f32_16x16x32_bf16 v[60:63], a[0:3], v[16:19], 0\n"
" ds_read_b128 v[36:39], v1 offset:21504\n"
" v_mfma_f32_16x16x32_bf16 v[64:67], a[4:7], v[16:19], 0\n"
" buffer_load_dwordx4 v1, s[16:19], s69 offen offset:2048 sc1 lds\n"
" v_mfma_f32_16x16x32_bf16 v[68:71], a[8:11], v[16:19], 0\n"
" v_mfma_f32_16x16x32_bf16 v[60:63], a[12:15], v[20:23], v[60:63]\n"
" ds_read_b128 v[40:43], v1 offset:22528\n"
" s_waitcnt lgkmcnt(5)\n"
" v_fmac_f32 v48, v208, v96\n"
" v_mfma_f32_16x16x32_bf16 v[64:67], a[16:19], v[20:23], v[64:67]\n"
" v_fmac_f32 v49, v209, v96\n"
" v_fmac_f32 v50, v210, v96\n"
" v_mfma_f32_16x16x32_bf16 v[68:71], a[20:23], v[20:23], v[68:71]\n"
" buffer_load_dwordx4 v1, s[16:19], s69 offen offset:3072 sc1 lds\n"
" s_add_u32 s69, s69, 16384\n"
" s_add_u32 m0, m0, 16384\n"
" v_mfma_f32_16x16x32_bf16 v[60:63], a[24:27], v[24:27], v[60:63]\n"
" ds_read_b128 v[44:47], v1 offset:23552\n"
" v_fmac_f32 v51, v211, v96\n"
" v_mfma_f32_16x16x32_bf16 v[64:67], a[28:31], v[24:27], v[64:67]\n"
" v_fmac_f32 v48, v220, v97\n"
" v_fmac_f32 v49, v221, v97\n"
" v_mfma_f32_16x16x32_bf16 v[68:71], a[32:35], v[24:27], v[68:71]\n"
" v_fmac_f32 v50, v222, v97\n"
" v_fmac_f32 v51, v223, v97\n"
" v_mfma_f32_16x16x32_bf16 v[60:63], a[36:39], v[28:31], v[60:63]\n"
" ds_read_b128 v[16:19], v1 offset:24576\n"
" v_max_f32 v48, 0, v48\n"
" v_mfma_f32_16x16x32_bf16 v[64:67], a[40:43], v[28:31], v[64:67]\n"
" buffer_load_dwordx4 v1, s[16:19], s69 offen offset:0 sc1 lds\n"
" v_max_f32 v49, 0, v49\n"
" v_mfma_f32_16x16x32_bf16 v[68:71], a[44:47], v[28:31], v[68:71]\n"
" s_waitcnt lgkmcnt(3)\n"
" v_mul_f32 v50, v98, v50\n"
" v_mul_f32 v51, v98, v51\n"
" v_mfma_f32_16x16x32_bf16 v[60:63], a[48:51], v[32:35], v[60:63]\n"
" ds_read_b128 v[20:23], v1 offset:25600\n"
" v_mul_f32 v48, v48, v50\n"
" v_mfma_f32_16x16x32_bf16 v[64:67], a[52:55], v[32:35], v[64:67]\n"
" v_mul_f32 v49, v49, v51\n"
" v_cvt_pk_bf16_f32 v112, v48, v49\n"
" v_mfma_f32_16x16x32_bf16 v[68:71], a[56:59], v[32:35], v[68:71]\n"
" v_fmac_f32 v52, v212, v96\n"
" v_fmac_f32 v53, v213, v96\n"
" v_mfma_f32_16x16x32_bf16 v[60:63], a[60:63], v[36:39], v[60:63]\n"
" ds_read_b128 v[24:27], v1 offset:26624\n"
" v_fmac_f32 v54, v214, v96\n"
" v_mfma_f32_16x16x32_bf16 v[64:67], a[64:67], v[36:39], v[64:67]\n"
" buffer_load_dwordx4 v1, s[16:19], s69 offen offset:1024 sc1 lds\n"
" v_fmac_f32 v55, v215, v96\n"
" v_mfma_f32_16x16x32_bf16 v[68:71], a[68:71], v[36:39], v[68:71]\n"
" s_waitcnt lgkmcnt(3)\n"
" v_fmac_f32 v52, v224, v97\n"
" v_fmac_f32 v53, v225, v97\n"
" v_mfma_f32_16x16x32_bf16 v[60:63], a[72:75], v[40:43], v[60:63]\n"
" ds_read_b128 v[28:31], v1 offset:27648\n"
" v_fmac_f32 v54, v226, v97\n"
" v_mfma_f32_16x16x32_bf16 v[64:67], a[76:79], v[40:43], v[64:67]\n"
" v_fmac_f32 v55, v227, v97\n"
" v_max_f32 v52, 0, v52\n"
" v_mfma_f32_16x16x32_bf16 v[68:71], a[80:83], v[40:43], v[68:71]\n"
" buffer_load_dwordx4 v1, s[16:19], s69 offen offset:2048 sc1 lds\n"
" v_max_f32 v53, 0, v53\n"
" v_mfma_f32_16x16x32_bf16 v[60:63], a[84:87], v[44:47], v[60:63]\n"
" ds_read_b128 v[32:35], v1 offset:28672\n"
" v_mul_f32 v54, v98, v54\n"
" v_mfma_f32_16x16x32_bf16 v[64:67], a[88:91], v[44:47], v[64:67]\n"
" v_mul_f32 v55, v98, v55\n"
" v_mul_f32 v52, v52, v54\n"
" v_mfma_f32_16x16x32_bf16 v[68:71], a[92:95], v[44:47], v[68:71]\n"
" s_waitcnt lgkmcnt(3)\n"
" v_mul_f32 v53, v53, v55\n"
" v_cvt_pk_bf16_f32 v113, v52, v53\n"
" v_mfma_f32_16x16x32_bf16 v[60:63], a[96:99], v[16:19], v[60:63]\n"
" ds_read_b128 v[36:39], v1 offset:29696\n"
" v_fmac_f32 v56, v216, v96\n"
" v_mfma_f32_16x16x32_bf16 v[64:67], a[100:103], v[16:19], v[64:67]\n"
" buffer_load_dwordx4 v1, s[16:19], s69 offen offset:3072 sc1 lds\n"
" s_add_u32 s69, s69, 16384\n"
" s_add_u32 m0, m0, 16384\n"
" v_mfma_f32_16x16x32_bf16 v[68:71], a[104:107], v[16:19], v[68:71]\n"
" v_fmac_f32 v57, v217, v96\n"
" v_fmac_f32 v58, v218, v96\n"
" v_mfma_f32_16x16x32_bf16 v[60:63], a[108:111], v[20:23], v[60:63]\n"
" ds_read_b128 v[40:43], v1 offset:30720\n"
" v_fmac_f32 v59, v219, v96\n"
" v_mfma_f32_16x16x32_bf16 v[64:67], a[112:115], v[20:23], v[64:67]\n"
" v_fmac_f32 v56, v228, v97\n"
" v_fmac_f32 v57, v229, v97\n"
" v_mfma_f32_16x16x32_bf16 v[68:71], a[116:119], v[20:23], v[68:71]\n"
" s_waitcnt lgkmcnt(3)\n"
" buffer_load_dwordx4 v1, s[16:19], s69 offen offset:0 sc1 lds\n"
" v_fmac_f32 v58, v230, v97\n"
" v_mfma_f32_16x16x32_bf16 v[60:63], a[120:123], v[24:27], v[60:63]\n"
" ds_read_b128 v[44:47], v1 offset:31744\n"
" v_fmac_f32 v59, v231, v97\n"
" v_mfma_f32_16x16x32_bf16 v[64:67], a[124:127], v[24:27], v[64:67]\n"
" v_max_f32 v56, 0, v56\n"
" v_max_f32 v57, 0, v57\n"
" v_mfma_f32_16x16x32_bf16 v[68:71], a[128:131], v[24:27], v[68:71]\n"
" s_waitcnt vmcnt(9)\n"
" v_mul_f32 v58, v98, v58\n"
" s_barrier\n"
" v_mfma_f32_16x16x32_bf16 v[60:63], a[132:135], v[28:31], v[60:63]\n"
" ds_read_b128 v[16:19], v1 offset:32768\n"
" v_mul_f32 v59, v98, v59\n"
" v_mfma_f32_16x16x32_bf16 v[64:67], a[136:139], v[28:31], v[64:67]\n"
" buffer_load_dwordx4 v1, s[16:19], s69 offen offset:1024 sc1 lds\n"
" v_mul_f32 v56, v56, v58\n"
" v_mfma_f32_16x16x32_bf16 v[68:71], a[140:143], v[28:31], v[68:71]\n"
" s_waitcnt lgkmcnt(3)\n"
" v_mul_f32 v57, v57, v59\n"
" v_cvt_pk_bf16_f32 v116, v56, v57\n"
" v_mfma_f32_16x16x32_bf16 v[60:63], a[144:147], v[32:35], v[60:63]\n"
" ds_read_b128 v[20:23], v1 offset:33792\n"
" v_mul_f32 v72, v208, v104\n"
" v_mfma_f32_16x16x32_bf16 v[64:67], a[148:151], v[32:35], v[64:67]\n"
" v_mul_f32 v73, v209, v104\n"
" v_mul_f32 v74, v210, v104\n"
" v_mfma_f32_16x16x32_bf16 v[68:71], a[152:155], v[32:35], v[68:71]\n"
" v_mul_f32 v75, v211, v104\n"
" v_fmac_f32 v72, v220, v105\n"
" v_mfma_f32_16x16x32_bf16 v[60:63], a[156:159], v[36:39], v[60:63]\n"
" ds_read_b128 v[24:27], v1 offset:34816\n"
" v_fmac_f32 v73, v221, v105\n"
" v_mfma_f32_16x16x32_bf16 v[64:67], a[160:163], v[36:39], v[64:67]\n"
" buffer_load_dwordx4 v1, s[16:19], s69 offen offset:2048 sc1 lds\n"
" v_fmac_f32 v74, v222, v105\n"
" v_mfma_f32_16x16x32_bf16 v[68:71], a[164:167], v[36:39], v[68:71]\n"
" s_waitcnt lgkmcnt(3)\n"
" v_fmac_f32 v75, v223, v105\n"
" v_mul_f32 v76, v212, v104\n"
" v_mfma_f32_16x16x32_bf16 v[60:63], a[168:171], v[40:43], v[60:63]\n"
" ds_read_b128 v[28:31], v1 offset:35840\n"
" v_mul_f32 v77, v213, v104\n"
" v_mfma_f32_16x16x32_bf16 v[64:67], a[172:175], v[40:43], v[64:67]\n"
" v_mul_f32 v78, v214, v104\n"
" v_mul_f32 v79, v215, v104\n"
" v_mfma_f32_16x16x32_bf16 v[68:71], a[176:179], v[40:43], v[68:71]\n"
" buffer_load_dwordx4 v1, s[16:19], s69 offen offset:3072 sc1 lds\n"
" s_add_u32 s69, s69, 16384\n"
" s_add_u32 m0, m0, 16384\n"
" v_mfma_f32_16x16x32_bf16 v[60:63], a[180:183], v[44:47], v[60:63]\n"
" ds_read_b128 v[32:35], v1 offset:36864\n"
" v_fmac_f32 v76, v224, v105\n"
" v_mfma_f32_16x16x32_bf16 v[64:67], a[184:187], v[44:47], v[64:67]\n"
" v_fmac_f32 v77, v225, v105\n"
" v_fmac_f32 v78, v226, v105\n"
" v_mfma_f32_16x16x32_bf16 v[68:71], a[188:191], v[44:47], v[68:71]\n"
" v_fmac_f32 v79, v227, v105\n"
" v_mul_f32 v80, v216, v104\n"
" v_mul_f32 v81, v217, v104\n"
" v_mul_f32 v82, v218, v104\n"
" v_mul_f32 v83, v219, v104\n"
" v_fmac_f32 v80, v228, v105\n"
" v_fmac_f32 v81, v229, v105\n"
" v_fmac_f32 v82, v230, v105\n"
" v_fmac_f32 v83, v231, v105\n"
" ds_read_b128 v[104:107], v11 offset:512\n"
" s_waitcnt lgkmcnt(4)\n"
" v_mfma_f32_16x16x32_bf16 v[48:51], a[0:3], v[16:19], 0\n"
" ds_read_b128 v[36:39], v1 offset:37888\n"
" v_mfma_f32_16x16x32_bf16 v[52:55], a[4:7], v[16:19], 0\n"
" buffer_load_dwordx4 v1, s[16:19], s69 offen offset:0 sc1 lds\n"
" v_mfma_f32_16x16x32_bf16 v[56:59], a[8:11], v[16:19], 0\n"
" v_mfma_f32_16x16x32_bf16 v[48:51], a[12:15], v[20:23], v[48:51]\n"
" ds_read_b128 v[40:43], v1 offset:38912\n"
" v_fmac_f32 v60, v208, v100\n"
" v_mfma_f32_16x16x32_bf16 v[52:55], a[16:19], v[20:23], v[52:55]\n"
" v_fmac_f32 v61, v209, v100\n"
" v_fmac_f32 v62, v210, v100\n"
" v_mfma_f32_16x16x32_bf16 v[56:59], a[20:23], v[20:23], v[56:59]\n"
" s_waitcnt lgkmcnt(4)\n"
" buffer_load_dwordx4 v1, s[16:19], s69 offen offset:1024 sc1 lds\n"
" v_fmac_f32 v63, v211, v100\n"
" v_mfma_f32_16x16x32_bf16 v[48:51], a[24:27], v[24:27], v[48:51]\n"
" ds_read_b128 v[44:47], v1 offset:39936\n"
" v_fmac_f32 v60, v220, v101\n"
" v_mfma_f32_16x16x32_bf16 v[52:55], a[28:31], v[24:27], v[52:55]\n"
" v_fmac_f32 v61, v221, v101\n"
" v_fmac_f32 v62, v222, v101\n"
" v_mfma_f32_16x16x32_bf16 v[56:59], a[32:35], v[24:27], v[56:59]\n"
" v_fmac_f32 v63, v223, v101\n"
" v_max_f32 v60, 0, v60\n"
" v_mfma_f32_16x16x32_bf16 v[48:51], a[36:39], v[28:31], v[48:51]\n"
" ds_read_b128 v[16:19], v1 offset:40960\n"
" v_max_f32 v61, 0, v61\n"
" v_mfma_f32_16x16x32_bf16 v[52:55], a[40:43], v[28:31], v[52:55]\n"
" buffer_load_dwordx4 v1, s[16:19], s69 offen offset:2048 sc1 lds\n"
" v_mul_f32 v62, v102, v62\n"
" v_mfma_f32_16x16x32_bf16 v[56:59], a[44:47], v[28:31], v[56:59]\n"
" s_waitcnt lgkmcnt(3)\n"
" v_mul_f32 v63, v102, v63\n"
" v_mul_f32 v60, v60, v62\n"
" v_mfma_f32_16x16x32_bf16 v[48:51], a[48:51], v[32:35], v[48:51]\n"
" ds_read_b128 v[20:23], v1 offset:41984\n"
" v_mul_f32 v61, v61, v63\n"
" v_mfma_f32_16x16x32_bf16 v[52:55], a[52:55], v[32:35], v[52:55]\n"
" v_cvt_pk_bf16_f32 v114, v60, v61\n"
" v_fmac_f32 v64, v212, v100\n"
" v_mfma_f32_16x16x32_bf16 v[56:59], a[56:59], v[32:35], v[56:59]\n"
" v_fmac_f32 v65, v213, v100\n"
" v_fmac_f32 v66, v214, v100\n"
" v_mfma_f32_16x16x32_bf16 v[48:51], a[60:63], v[36:39], v[48:51]\n"
" ds_read_b128 v[24:27], v1 offset:43008\n"
" v_fmac_f32 v67, v215, v100\n"
" v_mfma_f32_16x16x32_bf16 v[52:55], a[64:67], v[36:39], v[52:55]\n"
" buffer_load_dwordx4 v1, s[16:19], s69 offen offset:3072 sc1 lds\n"
" v_fmac_f32 v64, v224, v101\n"
" v_mfma_f32_16x16x32_bf16 v[56:59], a[68:71], v[36:39], v[56:59]\n"
" s_waitcnt lgkmcnt(3)\n"
" v_fmac_f32 v65, v225, v101\n"
" v_fmac_f32 v66, v226, v101\n"
" v_mfma_f32_16x16x32_bf16 v[48:51], a[72:75], v[40:43], v[48:51]\n"
" ds_read_b128 v[28:31], v1 offset:44032\n"
" v_fmac_f32 v67, v227, v101\n"
" v_mfma_f32_16x16x32_bf16 v[52:55], a[76:79], v[40:43], v[52:55]\n"
" v_max_f32 v64, 0, v64\n"
" v_max_f32 v65, 0, v65\n"
" v_mfma_f32_16x16x32_bf16 v[56:59], a[80:83], v[40:43], v[56:59]\n"
" s_add_u32 s95, s88, 16384\n"
" buffer_load_dwordx4 a[224:227], v1, s[16:19], s95 offen offset:0 sc1\n"
" v_mul_f32 v66, v102, v66\n"
" v_mfma_f32_16x16x32_bf16 v[48:51], a[84:87], v[44:47], v[48:51]\n"
" ds_read_b128 v[32:35], v1 offset:45056\n"
" v_mul_f32 v67, v102, v67\n"
" v_mfma_f32_16x16x32_bf16 v[52:55], a[88:91], v[44:47], v[52:55]\n"
" v_mul_f32 v64, v64, v66\n"
" v_mul_f32 v65, v65, v67\n"
" v_mfma_f32_16x16x32_bf16 v[56:59], a[92:95], v[44:47], v[56:59]\n"
" s_waitcnt lgkmcnt(3)\n"
" v_cvt_pk_bf16_f32 v115, v64, v65\n"
" v_mfma_f32_16x16x32_bf16 v[48:51], a[96:99], v[16:19], v[48:51]\n"
" ds_read_b128 v[36:39], v1 offset:46080\n"
" v_mfma_f32_16x16x32_bf16 v[52:55], a[100:103], v[16:19], v[52:55]\n"
" buffer_load_dwordx4 a[228:231], v1, s[16:19], s95 offen offset:1024 sc1\n"
" v_mfma_f32_16x16x32_bf16 v[56:59], a[104:107], v[16:19], v[56:59]\n"
" v_permlane32_swap_b32 v112, v114\n"
" v_mfma_f32_16x16x32_bf16 v[48:51], a[108:111], v[20:23], v[48:51]\n"
" ds_read_b128 v[40:43], v1 offset:47104\n"
" v_mfma_f32_16x16x32_bf16 v[52:55], a[112:115], v[20:23], v[52:55]\n"
" s_add_u32 s51, s94, 0\n"
" v_permlane32_swap_b32 v113, v115\n"
" v_mfma_f32_16x16x32_bf16 v[56:59], a[116:119], v[20:23], v[56:59]\n"
" s_waitcnt lgkmcnt(3)\n"
" buffer_load_dwordx4 a[232:235], v1, s[16:19], s95 offen offset:2048 sc1\n"
" v_mfma_f32_16x16x32_bf16 v[48:51], a[120:123], v[24:27], v[48:51]\n"
" ds_read_b128 v[44:47], v1 offset:48128\n"
" v_mfma_f32_16x16x32_bf16 v[52:55], a[124:127], v[24:27], v[52:55]\n"
" buffer_store_dwordx4 v[112:115], v9, s[16:19], s51 offen sc1\n"
" v_mfma_f32_16x16x32_bf16 v[56:59], a[128:131], v[24:27], v[56:59]\n"
" v_fmac_f32 v68, v216, v100\n"
" v_fmac_f32 v69, v217, v100\n"
" v_mfma_f32_16x16x32_bf16 v[48:51], a[132:135], v[28:31], v[48:51]\n"
" ds_read_b128 v[16:19], v1 offset:49152\n"
" v_fmac_f32 v70, v218, v100\n"
" v_mfma_f32_16x16x32_bf16 v[52:55], a[136:139], v[28:31], v[52:55]\n"
" buffer_load_dwordx4 a[236:239], v1, s[16:19], s95 offen offset:3072 sc1\n"
" v_fmac_f32 v71, v219, v100\n"
" v_mfma_f32_16x16x32_bf16 v[56:59], a[140:143], v[28:31], v[56:59]\n"
" s_waitcnt lgkmcnt(3)\n"
" v_fmac_f32 v68, v228, v101\n"
" v_fmac_f32 v69, v229, v101\n"
" v_mfma_f32_16x16x32_bf16 v[48:51], a[144:147], v[32:35], v[48:51]\n"
" ds_read_b128 v[20:23], v1 offset:50176\n"
" v_fmac_f32 v70, v230, v101\n"
" v_mfma_f32_16x16x32_bf16 v[52:55], a[148:151], v[32:35], v[52:55]\n"
" v_fmac_f32 v71, v231, v101\n"
" v_max_f32 v68, 0, v68\n"
" v_mfma_f32_16x16x32_bf16 v[56:59], a[152:155], v[32:35], v[56:59]\n"
" v_max_f32 v69, 0, v69\n"
" v_mul_f32 v70, v102, v70\n"
" v_mfma_f32_16x16x32_bf16 v[48:51], a[156:159], v[36:39], v[48:51]\n"
" ds_read_b128 v[24:27], v1 offset:51200\n"
" v_mul_f32 v71, v102, v71\n"
" v_mfma_f32_16x16x32_bf16 v[52:55], a[160:163], v[36:39], v[52:55]\n"
" s_add_u32 s95, s88, 32768\n"
" buffer_load_dwordx4 a[240:243], v1, s[16:19], s95 offen offset:0 sc1\n"
" v_mul_f32 v68, v68, v70\n"
" v_mfma_f32_16x16x32_bf16 v[56:59], a[164:167], v[36:39], v[56:59]\n"
" s_waitcnt lgkmcnt(3)\n"
" v_mul_f32 v69, v69, v71\n"
" v_cvt_pk_bf16_f32 v118, v68, v69\n"
" v_mfma_f32_16x16x32_bf16 v[48:51], a[168:171], v[40:43], v[48:51]\n"
" ds_read_b128 v[28:31], v1 offset:52224\n"
" v_mfma_f32_16x16x32_bf16 v[52:55], a[172:175], v[40:43], v[52:55]\n"
" v_permlane32_swap_b32 v116, v118\n"
" v_mfma_f32_16x16x32_bf16 v[56:59], a[176:179], v[40:43], v[56:59]\n"
" buffer_load_dwordx4 a[244:247], v1, s[16:19], s95 offen offset:1024 sc1\n"
" v_mov_b32 v117, v116\n"
" v_mfma_f32_16x16x32_bf16 v[48:51], a[180:183], v[44:47], v[48:51]\n"
" ds_read_b128 v[32:35], v1 offset:53248\n"
" v_mov_b32 v119, v118\n"
" v_mfma_f32_16x16x32_bf16 v[52:55], a[184:187], v[44:47], v[52:55]\n"
" v_permlane16_swap_b32 v116, v117\n"
" v_mfma_f32_16x16x32_bf16 v[56:59], a[188:191], v[44:47], v[56:59]\n"
" s_add_u32 s51, s56, 0\n"
" v_permlane16_swap_b32 v118, v119\n"
" s_mov_b64 exec, s[52:53]\n"
" buffer_store_dwordx4 v[116:119], v9, s[16:19], s51 offen sc1\n"
" s_mov_b64 exec, -1\n"
" ds_read_b128 v[96:99], v11 offset:1024\n"
" s_waitcnt lgkmcnt(4)\n"
" v_mfma_f32_16x16x32_bf16 v[60:63], a[0:3], v[16:19], 0\n"
" ds_read_b128 v[36:39], v1 offset:54272\n"
" v_mfma_f32_16x16x32_bf16 v[64:67], a[4:7], v[16:19], 0\n"
" buffer_load_dwordx4 a[248:251], v1, s[16:19], s95 offen offset:2048 sc1\n"
" v_mfma_f32_16x16x32_bf16 v[68:71], a[8:11], v[16:19], 0\n"
" v_mfma_f32_16x16x32_bf16 v[60:63], a[12:15], v[20:23], v[60:63]\n"
" ds_read_b128 v[40:43], v1 offset:55296\n"
" v_fmac_f32 v48, v208, v104\n"
" v_mfma_f32_16x16x32_bf16 v[64:67], a[16:19], v[20:23], v[64:67]\n"
" v_fmac_f32 v49, v209, v104\n"
" v_fmac_f32 v50, v210, v104\n"
" v_mfma_f32_16x16x32_bf16 v[68:71], a[20:23], v[20:23], v[68:71]\n"
" s_waitcnt lgkmcnt(4)\n"
" buffer_load_dwordx4 a[252:255], v1, s[16:19], s95 offen offset:3072 sc1\n"
" v_fmac_f32 v51, v211, v104\n"
" v_mfma_f32_16x16x32_bf16 v[60:63], a[24:27], v[24:27], v[60:63]\n"
" ds_read_b128 v[44:47], v1 offset:56320\n"
" v_fmac_f32 v48, v220, v105\n"
" v_mfma_f32_16x16x32_bf16 v[64:67], a[28:31], v[24:27], v[64:67]\n"
" v_fmac_f32 v49, v221, v105\n"
" v_fmac_f32 v50, v222, v105\n"
" v_mfma_f32_16x16x32_bf16 v[68:71], a[32:35], v[24:27], v[68:71]\n"
" v_fmac_f32 v51, v223, v105\n"
" v_max_f32 v48, 0, v48\n"
" v_mfma_f32_16x16x32_bf16 v[60:63], a[36:39], v[28:31], v[60:63]\n"
" ds_read_b128 v[16:19], v1 offset:57344\n"
" v_max_f32 v49, 0, v49\n"
" v_mfma_f32_16x16x32_bf16 v[64:67], a[40:43], v[28:31], v[64:67]\n"
" s_add_u32 s95, s88, 49152\n"
" buffer_load_dwordx4 v[128:131], v1, s[16:19], s95 offen offset:0 sc1\n"
" v_mul_f32 v50, v106, v50\n"
" v_mfma_f32_16x16x32_bf16 v[68:71], a[44:47], v[28:31], v[68:71]\n"
" s_waitcnt lgkmcnt(3)\n"
" v_mul_f32 v51, v106, v51\n"
" v_mul_f32 v48, v48, v50\n"
" v_mfma_f32_16x16x32_bf16 v[60:63], a[48:51], v[32:35], v[60:63]\n"
" ds_read_b128 v[20:23], v1 offset:58368\n"
" v_mul_f32 v49, v49, v51\n"
" v_mfma_f32_16x16x32_bf16 v[64:67], a[52:55], v[32:35], v[64:67]\n"
" v_cvt_pk_bf16_f32 v112, v48, v49\n"
" v_fmac_f32 v52, v212, v104\n"
" v_mfma_f32_16x16x32_bf16 v[68:71], a[56:59], v[32:35], v[68:71]\n"
" v_fmac_f32 v53, v213, v104\n"
" v_fmac_f32 v54, v214, v104\n"
" v_mfma_f32_16x16x32_bf16 v[60:63], a[60:63], v[36:39], v[60:63]\n"
" ds_read_b128 v[24:27], v1 offset:59392\n"
" v_fmac_f32 v55, v215, v104\n"
" v_mfma_f32_16x16x32_bf16 v[64:67], a[64:67], v[36:39], v[64:67]\n"
" buffer_load_dwordx4 v[132:135], v1, s[16:19], s95 offen offset:1024 sc1\n"
" v_fmac_f32 v52, v224, v105\n"
" v_mfma_f32_16x16x32_bf16 v[68:71], a[68:71], v[36:39], v[68:71]\n"
" s_waitcnt lgkmcnt(3)\n"
" v_fmac_f32 v53, v225, v105\n"
" v_fmac_f32 v54, v226, v105\n"
" v_mfma_f32_16x16x32_bf16 v[60:63], a[72:75], v[40:43], v[60:63]\n"
" ds_read_b128 v[28:31], v1 offset:60416\n"
" v_fmac_f32 v55, v227, v105\n"
" v_mfma_f32_16x16x32_bf16 v[64:67], a[76:79], v[40:43], v[64:67]\n"
" v_max_f32 v52, 0, v52\n"
" v_max_f32 v53, 0, v53\n"
" v_mfma_f32_16x16x32_bf16 v[68:71], a[80:83], v[40:43], v[68:71]\n"
" buffer_load_dwordx4 v[136:139], v1, s[16:19], s95 offen offset:2048 sc1\n"
" v_mul_f32 v54, v106, v54\n"
" v_mfma_f32_16x16x32_bf16 v[60:63], a[84:87], v[44:47], v[60:63]\n"
" ds_read_b128 v[32:35], v1 offset:61440\n"
" v_mul_f32 v55, v106, v55\n"
" v_mfma_f32_16x16x32_bf16 v[64:67], a[88:91], v[44:47], v[64:67]\n"
" v_mul_f32 v52, v52, v54\n"
" v_mul_f32 v53, v53, v55\n"
" v_mfma_f32_16x16x32_bf16 v[68:71], a[92:95], v[44:47], v[68:71]\n"
" s_waitcnt lgkmcnt(3)\n"
" v_cvt_pk_bf16_f32 v113, v52, v53\n"
" v_fmac_f32 v56, v216, v104\n"
" v_mfma_f32_16x16x32_bf16 v[60:63], a[96:99], v[16:19], v[60:63]\n"
" ds_read_b128 v[36:39], v1 offset:62464\n"
" v_fmac_f32 v57, v217, v104\n"
" v_mfma_f32_16x16x32_bf16 v[64:67], a[100:103], v[16:19], v[64:67]\n"
" buffer_load_dwordx4 v[140:143], v1, s[16:19], s95 offen offset:3072 sc1\n"
" v_fmac_f32 v58, v218, v104\n"
" v_mfma_f32_16x16x32_bf16 v[68:71], a[104:107], v[16:19], v[68:71]\n"
" v_fmac_f32 v59, v219, v104\n"
" v_fmac_f32 v56, v228, v105\n"
" v_mfma_f32_16x16x32_bf16 v[60:63], a[108:111], v[20:23], v[60:63]\n"
" ds_read_b128 v[40:43], v1 offset:63488\n"
" v_fmac_f32 v57, v229, v105\n"
" v_mfma_f32_16x16x32_bf16 v[64:67], a[112:115], v[20:23], v[64:67]\n"
" v_fmac_f32 v58, v230, v105\n"
" v_fmac_f32 v59, v231, v105\n"
" v_mfma_f32_16x16x32_bf16 v[68:71], a[116:119], v[20:23], v[68:71]\n"
" s_add_u32 s95, s88, 65536\n"
" buffer_load_dwordx4 v[144:147], v1, s[16:19], s95 offen offset:0 sc1\n"
" s_waitcnt lgkmcnt(3)\n"
" v_mfma_f32_16x16x32_bf16 v[60:63], a[120:123], v[24:27], v[60:63]\n"
" ds_read_b128 v[44:47], v1 offset:64512\n"
" v_max_f32 v56, 0, v56\n"
" v_mfma_f32_16x16x32_bf16 v[64:67], a[124:127], v[24:27], v[64:67]\n"
" v_max_f32 v57, 0, v57\n"
" v_mul_f32 v58, v106, v58\n"
" v_mfma_f32_16x16x32_bf16 v[68:71], a[128:131], v[24:27], v[68:71]\n"
" s_waitcnt vmcnt(15)\n"
" v_mul_f32 v59, v106, v59\n"
" s_barrier\n"
" v_mfma_f32_16x16x32_bf16 v[60:63], a[132:135], v[28:31], v[60:63]\n"
" ds_read_b128 v[16:19], v8 offset:0\n"
" v_mul_f32 v56, v56, v58\n"
" v_mfma_f32_16x16x32_bf16 v[64:67], a[136:139], v[28:31], v[64:67]\n"
" buffer_load_dwordx4 v[148:151], v1, s[16:19], s95 offen offset:1024 sc1\n"
" v_mul_f32 v57, v57, v59\n"
" v_mfma_f32_16x16x32_bf16 v[68:71], a[140:143], v[28:31], v[68:71]\n"
" s_waitcnt lgkmcnt(3)\n"
" v_cvt_pk_bf16_f32 v116, v56, v57\n"
" v_mul_f32 v84, v208, v108\n"
" v_mfma_f32_16x16x32_bf16 v[60:63], a[144:147], v[32:35], v[60:63]\n"
" ds_read_b128 v[20:23], v8 offset:1024\n"
" v_mul_f32 v85, v209, v108\n"
" v_mfma_f32_16x16x32_bf16 v[64:67], a[148:151], v[32:35], v[64:67]\n"
" v_mul_f32 v86, v210, v108\n"
" v_mul_f32 v87, v211, v108\n"
" v_mfma_f32_16x16x32_bf16 v[68:71], a[152:155], v[32:35], v[68:71]\n"
" v_fmac_f32 v84, v220, v109\n"
" v_fmac_f32 v85, v221, v109\n"
" v_mfma_f32_16x16x32_bf16 v[60:63], a[156:159], v[36:39], v[60:63]\n"
" ds_read_b128 v[24:27], v8 offset:2048\n"
" v_fmac_f32 v86, v222, v109\n"
" v_mfma_f32_16x16x32_bf16 v[64:67], a[160:163], v[36:39], v[64:67]\n"
" buffer_load_dwordx4 v[152:155], v1, s[16:19], s95 offen offset:2048 sc1\n"
" v_fmac_f32 v87, v223, v109\n"
" v_mfma_f32_16x16x32_bf16 v[68:71], a[164:167], v[36:39], v[68:71]\n"
" s_waitcnt lgkmcnt(3)\n"
" v_mul_f32 v88, v212, v108\n"
" v_mul_f32 v89, v213, v108\n"
" v_mfma_f32_16x16x32_bf16 v[60:63], a[168:171], v[40:43], v[60:63]\n"
" ds_read_b128 v[28:31], v8 offset:3072\n"
" v_mul_f32 v90, v214, v108\n"
" v_mfma_f32_16x16x32_bf16 v[64:67], a[172:175], v[40:43], v[64:67]\n"
" v_mul_f32 v91, v215, v108\n"
" v_fmac_f32 v88, v224, v109\n"
" v_mfma_f32_16x16x32_bf16 v[68:71], a[176:179], v[40:43], v[68:71]\n"
" buffer_load_dwordx4 v[156:159], v1, s[16:19], s95 offen offset:3072 sc1\n"
" v_fmac_f32 v89, v225, v109\n"
" v_mfma_f32_16x16x32_bf16 v[60:63], a[180:183], v[44:47], v[60:63]\n"
" ds_read_b128 v[32:35], v8 offset:4096\n"
" v_fmac_f32 v90, v226, v109\n"
" v_mfma_f32_16x16x32_bf16 v[64:67], a[184:187], v[44:47], v[64:67]\n"
" v_fmac_f32 v91, v227, v109\n"
" v_mul_f32 v92, v216, v108\n"
" v_mfma_f32_16x16x32_bf16 v[68:71], a[188:191], v[44:47], v[68:71]\n"
" v_mul_f32 v93, v217, v108\n"
" v_mul_f32 v94, v218, v108\n"
" v_mul_f32 v95, v219, v108\n"
" v_fmac_f32 v92, v228, v109\n"
" v_fmac_f32 v93, v229, v109\n"
" v_fmac_f32 v94, v230, v109\n"
" v_fmac_f32 v95, v231, v109\n"
" ds_read_b128 v[108:111], v11 offset:768\n"
" ds_read_b128 v[100:103], v11 offset:1280\n"
" s_waitcnt lgkmcnt(5)\n"
" v_mfma_f32_16x16x32_bf16 v[48:51], a[0:3], v[16:19], 0\n"
" ds_read_b128 v[36:39], v8 offset:5120\n"
" v_mfma_f32_16x16x32_bf16 v[52:55], a[4:7], v[16:19], 0\n"
" s_add_u32 s95, s88, 81920\n"
" buffer_load_dwordx4 v[160:163], v1, s[16:19], s95 offen offset:0 sc1\n"
" v_mfma_f32_16x16x32_bf16 v[56:59], a[8:11], v[16:19], 0\n"
" v_mfma_f32_16x16x32_bf16 v[48:51], a[12:15], v[20:23], v[48:51]\n"
" ds_read_b128 v[40:43], v8 offset:6144\n"
" s_waitcnt lgkmcnt(3)\n"
" v_fmac_f32 v60, v208, v108\n"
" v_mfma_f32_16x16x32_bf16 v[52:55], a[16:19], v[20:23], v[52:55]\n"
" v_fmac_f32 v61, v209, v108\n"
" v_fmac_f32 v62, v210, v108\n"
" v_mfma_f32_16x16x32_bf16 v[56:59], a[20:23], v[20:23], v[56:59]\n"
" buffer_load_dwordx4 v[164:167], v1, s[16:19], s95 offen offset:1024 sc1\n"
" v_fmac_f32 v63, v211, v108\n"
" v_mfma_f32_16x16x32_bf16 v[48:51], a[24:27], v[24:27], v[48:51]\n"
" ds_read_b128 v[44:47], v8 offset:7168\n"
" v_fmac_f32 v60, v220, v109\n"
" v_mfma_f32_16x16x32_bf16 v[52:55], a[28:31], v[24:27], v[52:55]\n"
" v_fmac_f32 v61, v221, v109\n"
" v_fmac_f32 v62, v222, v109\n"
" v_mfma_f32_16x16x32_bf16 v[56:59], a[32:35], v[24:27], v[56:59]\n"
" v_fmac_f32 v63, v223, v109\n"
" v_max_f32 v60, 0, v60\n"
" v_mfma_f32_16x16x32_bf16 v[48:51], a[36:39], v[28:31], v[48:51]\n"
" ds_read_b128 v[16:19], v8 offset:8192\n"
" v_max_f32 v61, 0, v61\n"
" v_mfma_f32_16x16x32_bf16 v[52:55], a[40:43], v[28:31], v[52:55]\n"
" buffer_load_dwordx4 v[168:171], v1, s[16:19], s95 offen offset:2048 sc1\n"
" v_mul_f32 v62, v110, v62\n"
" v_mfma_f32_16x16x32_bf16 v[56:59], a[44:47], v[28:31], v[56:59]\n"
" s_waitcnt lgkmcnt(3)\n"
" v_mul_f32 v63, v110, v63\n"
" v_mul_f32 v60, v60, v62\n"
" v_mfma_f32_16x16x32_bf16 v[48:51], a[48:51], v[32:35], v[48:51]\n"
" ds_read_b128 v[20:23], v8 offset:9216\n"
" v_mul_f32 v61, v61, v63\n"
" v_mfma_f32_16x16x32_bf16 v[52:55], a[52:55], v[32:35], v[52:55]\n"
" v_cvt_pk_bf16_f32 v114, v60, v61\n"
" v_fmac_f32 v64, v212, v108\n"
" v_mfma_f32_16x16x32_bf16 v[56:59], a[56:59], v[32:35], v[56:59]\n"
" v_fmac_f32 v65, v213, v108\n"
" v_fmac_f32 v66, v214, v108\n"
" v_mfma_f32_16x16x32_bf16 v[48:51], a[60:63], v[36:39], v[48:51]\n"
" ds_read_b128 v[24:27], v8 offset:10240\n"
" v_fmac_f32 v67, v215, v108\n"
" v_mfma_f32_16x16x32_bf16 v[52:55], a[64:67], v[36:39], v[52:55]\n"
" buffer_load_dwordx4 v[172:175], v1, s[16:19], s95 offen offset:3072 sc1\n"
" v_fmac_f32 v64, v224, v109\n"
" v_mfma_f32_16x16x32_bf16 v[56:59], a[68:71], v[36:39], v[56:59]\n"
" s_waitcnt lgkmcnt(3)\n"
" v_fmac_f32 v65, v225, v109\n"
" v_fmac_f32 v66, v226, v109\n"
" v_mfma_f32_16x16x32_bf16 v[48:51], a[72:75], v[40:43], v[48:51]\n"
" ds_read_b128 v[28:31], v8 offset:11264\n"
" v_fmac_f32 v67, v227, v109\n"
" v_mfma_f32_16x16x32_bf16 v[52:55], a[76:79], v[40:43], v[52:55]\n"
" v_max_f32 v64, 0, v64\n"
" v_max_f32 v65, 0, v65\n"
" v_mfma_f32_16x16x32_bf16 v[56:59], a[80:83], v[40:43], v[56:59]\n"
" s_add_u32 s95, s88, 98304\n"
" buffer_load_dwordx4 v[176:179], v1, s[16:19], s95 offen offset:0 sc1\n"
" v_mul_f32 v66, v110, v66\n"
" v_mfma_f32_16x16x32_bf16 v[48:51], a[84:87], v[44:47], v[48:51]\n"
" ds_read_b128 v[32:35], v8 offset:12288\n"
" v_mul_f32 v67, v110, v67\n"
" v_mfma_f32_16x16x32_bf16 v[52:55], a[88:91], v[44:47], v[52:55]\n"
" v_mul_f32 v64, v64, v66\n"
" v_mul_f32 v65, v65, v67\n"
" v_mfma_f32_16x16x32_bf16 v[56:59], a[92:95], v[44:47], v[56:59]\n"
" s_waitcnt lgkmcnt(3)\n"
" v_cvt_pk_bf16_f32 v115, v64, v65\n"
" v_mfma_f32_16x16x32_bf16 v[48:51], a[96:99], v[16:19], v[48:51]\n"
" ds_read_b128 v[36:39], v8 offset:13312\n"
" v_mfma_f32_16x16x32_bf16 v[52:55], a[100:103], v[16:19], v[52:55]\n"
" buffer_load_dwordx4 v[180:183], v1, s[16:19], s95 offen offset:1024 sc1\n"
" v_mfma_f32_16x16x32_bf16 v[56:59], a[104:107], v[16:19], v[56:59]\n"
" v_permlane32_swap_b32 v112, v114\n"
" v_mfma_f32_16x16x32_bf16 v[48:51], a[108:111], v[20:23], v[48:51]\n"
" ds_read_b128 v[40:43], v8 offset:14336\n"
" v_mfma_f32_16x16x32_bf16 v[52:55], a[112:115], v[20:23], v[52:55]\n"
" s_add_u32 s51, s94, 88064\n"
" v_permlane32_swap_b32 v113, v115\n"
" v_mfma_f32_16x16x32_bf16 v[56:59], a[116:119], v[20:23], v[56:59]\n"
" s_waitcnt lgkmcnt(3)\n"
" buffer_store_dwordx4 v[112:115], v9, s[16:19], s51 offen sc1\n"
" v_mfma_f32_16x16x32_bf16 v[48:51], a[120:123], v[24:27], v[48:51]\n"
" ds_read_b128 v[44:47], v8 offset:15360\n"
" v_fmac_f32 v68, v216, v108\n"
" v_mfma_f32_16x16x32_bf16 v[52:55], a[124:127], v[24:27], v[52:55]\n"
" v_fmac_f32 v69, v217, v108\n"
" v_fmac_f32 v70, v218, v108\n"
" v_mfma_f32_16x16x32_bf16 v[56:59], a[128:131], v[24:27], v[56:59]\n"
" v_fmac_f32 v71, v219, v108\n"
" v_fmac_f32 v68, v228, v109\n"
" v_mfma_f32_16x16x32_bf16 v[48:51], a[132:135], v[28:31], v[48:51]\n"
" ds_read_b128 v[16:19], v8 offset:16384\n"
" v_fmac_f32 v69, v229, v109\n"
" v_mfma_f32_16x16x32_bf16 v[52:55], a[136:139], v[28:31], v[52:55]\n"
" v_fmac_f32 v70, v230, v109\n"
" v_fmac_f32 v71, v231, v109\n"
" v_mfma_f32_16x16x32_bf16 v[56:59], a[140:143], v[28:31], v[56:59]\n"
" s_waitcnt lgkmcnt(3)\n"
" v_max_f32 v68, 0, v68\n"
" v_max_f32 v69, 0, v69\n"
" v_mfma_f32_16x16x32_bf16 v[48:51], a[144:147], v[32:35], v[48:51]\n"
" ds_read_b128 v[20:23], v8 offset:17408\n"
" v_mul_f32 v70, v110, v70\n"
" v_mfma_f32_16x16x32_bf16 v[52:55], a[148:151], v[32:35], v[52:55]\n"
" v_mul_f32 v71, v110, v71\n"
" v_mul_f32 v68, v68, v70\n"
" v_mfma_f32_16x16x32_bf16 v[56:59], a[152:155], v[32:35], v[56:59]\n"
" v_mul_f32 v69, v69, v71\n"
" v_cvt_pk_bf16_f32 v118, v68, v69\n"
" v_mfma_f32_16x16x32_bf16 v[48:51], a[156:159], v[36:39], v[48:51]\n"
" ds_read_b128 v[24:27], v8 offset:18432\n"
" v_mfma_f32_16x16x32_bf16 v[52:55], a[160:163], v[36:39], v[52:55]\n"
" v_permlane32_swap_b32 v116, v118\n"
" v_mfma_f32_16x16x32_bf16 v[56:59], a[164:167], v[36:39], v[56:59]\n"
" s_waitcnt lgkmcnt(3)\n"
" v_mov_b32 v117, v116\n"
" v_mov_b32 v119, v118\n"
" v_mfma_f32_16x16x32_bf16 v[48:51], a[168:171], v[40:43], v[48:51]\n"
" ds_read_b128 v[28:31], v8 offset:19456\n"
" v_mfma_f32_16x16x32_bf16 v[52:55], a[172:175], v[40:43], v[52:55]\n"
" v_permlane16_swap_b32 v116, v117\n"
" v_mfma_f32_16x16x32_bf16 v[56:59], a[176:179], v[40:43], v[56:59]\n"
" s_add_u32 s51, s56, 88064\n"
" v_permlane16_swap_b32 v118, v119\n"
" v_mfma_f32_16x16x32_bf16 v[48:51], a[180:183], v[44:47], v[48:51]\n"
" s_mov_b64 exec, s[52:53]\n"
" buffer_store_dwordx4 v[116:119], v9, s[16:19], s51 offen sc1\n"
" s_mov_b64 exec, -1\n"
" ds_read_b128 v[32:35], v8 offset:20480\n"
" s_nop 0\n"
" v_mfma_f32_16x16x32_bf16 v[52:55], a[184:187], v[44:47], v[52:55]\n"
" v_mfma_f32_16x16x32_bf16 v[56:59], a[188:191], v[44:47], v[56:59]\n"
" s_waitcnt lgkmcnt(3)\n"
" v_mfma_f32_16x16x32_bf16 v[60:63], a[0:3], v[16:19], 0\n"
" ds_read_b128 v[36:39], v8 offset:21504\n"
" v_mfma_f32_16x16x32_bf16 v[64:67], a[4:7], v[16:19], 0\n"
" buffer_load_dwordx4 v[184:187], v1, s[16:19], s95 offen offset:2048 sc1\n"
" v_mfma_f32_16x16x32_bf16 v[68:71], a[8:11], v[16:19], 0\n"
" v_mfma_f32_16x16x32_bf16 v[60:63], a[12:15], v[20:23], v[60:63]\n"
" ds_read_b128 v[40:43], v8 offset:22528\n"
" v_fmac_f32 v48, v208, v96\n"
" v_mfma_f32_16x16x32_bf16 v[64:67], a[16:19], v[20:23], v[64:67]\n"
" v_fmac_f32 v49, v209, v96\n"
" v_fmac_f32 v50, v210, v96\n"
" v_mfma_f32_16x16x32_bf16 v[68:71], a[20:23], v[20:23], v[68:71]\n"
" s_waitcnt lgkmcnt(3)\n"
" buffer_load_dwordx4 v[188:191], v1, s[16:19], s95 offen offset:3072 sc1\n"
" v_fmac_f32 v51, v211, v96\n"
" v_mfma_f32_16x16x32_bf16 v[60:63], a[24:27], v[24:27], v[60:63]\n"
" ds_read_b128 v[44:47], v8 offset:23552\n"
" v_fmac_f32 v48, v220, v97\n"
" v_mfma_f32_16x16x32_bf16 v[64:67], a[28:31], v[24:27], v[64:67]\n"
" v_fmac_f32 v49, v221, v97\n"
" v_fmac_f32 v50, v222, v97\n"
" v_mfma_f32_16x16x32_bf16 v[68:71], a[32:35], v[24:27], v[68:71]\n"
" v_fmac_f32 v51, v223, v97\n"
" v_max_f32 v48, 0, v48\n"
" v_mfma_f32_16x16x32_bf16 v[60:63], a[36:39], v[28:31], v[60:63]\n"
" ds_read_b128 v[16:19], v8 offset:24576\n"
" v_max_f32 v49, 0, v49\n"
" v_mfma_f32_16x16x32_bf16 v[64:67], a[40:43], v[28:31], v[64:67]\n"
" s_add_u32 s95, s88, 114688\n"
" buffer_load_dwordx4 v[192:195], v1, s[16:19], s95 offen offset:0 sc1\n"
" v_mul_f32 v50, v98, v50\n"
" v_mfma_f32_16x16x32_bf16 v[68:71], a[44:47], v[28:31], v[68:71]\n"
" s_waitcnt lgkmcnt(3)\n"
" v_mul_f32 v51, v98, v51\n"
" v_mul_f32 v48, v48, v50\n"
" v_mfma_f32_16x16x32_bf16 v[60:63], a[48:51], v[32:35], v[60:63]\n"
" ds_read_b128 v[20:23], v8 offset:25600\n"
" v_mul_f32 v49, v49, v51\n"
" v_mfma_f32_16x16x32_bf16 v[64:67], a[52:55], v[32:35], v[64:67]\n"
" v_cvt_pk_bf16_f32 v112, v48, v49\n"
" v_fmac_f32 v52, v212, v96\n"
" v_mfma_f32_16x16x32_bf16 v[68:71], a[56:59], v[32:35], v[68:71]\n"
" v_fmac_f32 v53, v213, v96\n"
" v_fmac_f32 v54, v214, v96\n"
" v_mfma_f32_16x16x32_bf16 v[60:63], a[60:63], v[36:39], v[60:63]\n"
" ds_read_b128 v[24:27], v8 offset:26624\n"
" v_fmac_f32 v55, v215, v96\n"
" v_mfma_f32_16x16x32_bf16 v[64:67], a[64:67], v[36:39], v[64:67]\n"
" buffer_load_dwordx4 v[196:199], v1, s[16:19], s95 offen offset:1024 sc1\n"
" v_fmac_f32 v52, v224, v97\n"
" v_mfma_f32_16x16x32_bf16 v[68:71], a[68:71], v[36:39], v[68:71]\n"
" s_waitcnt lgkmcnt(3)\n"
" v_fmac_f32 v53, v225, v97\n"
" v_fmac_f32 v54, v226, v97\n"
" v_mfma_f32_16x16x32_bf16 v[60:63], a[72:75], v[40:43], v[60:63]\n"
" ds_read_b128 v[28:31], v8 offset:27648\n"
" v_fmac_f32 v55, v227, v97\n"
" v_mfma_f32_16x16x32_bf16 v[64:67], a[76:79], v[40:43], v[64:67]\n"
" v_max_f32 v52, 0, v52\n"
" v_max_f32 v53, 0, v53\n"
" v_mfma_f32_16x16x32_bf16 v[68:71], a[80:83], v[40:43], v[68:71]\n"
" buffer_load_dwordx4 v[200:203], v1, s[16:19], s95 offen offset:2048 sc1\n"
" s_waitcnt vmcnt(5)\n"
" s_barrier\n"
" s_cmp_eq_u32 s3, 3\n"
" s_cbranch_scc0 .LL_f3\n"
" s_lshl_b32 s51, s2, 7\n"
" s_add_u32 s51, s51, 491520\n"
" v_mov_b32 v232, s48\n"
" v_lshlrev_b32 v233, 2, v0\n"
" s_mov_b32 exec_hi, 0\n"
" buffer_store_dword v232, v233, s[16:19], s51 offen sc1\n"
" s_mov_b64 exec, -1\n"
".LL_f3:\n"
" s_nop 1\n"
" v_mfma_f32_16x16x32_bf16 v[60:63], a[84:87], v[44:47], v[60:63]\n"
" ds_read_b128 v[32:35], v8 offset:28672\n"
" v_mul_f32 v54, v98, v54\n"
" v_mfma_f32_16x16x32_bf16 v[64:67], a[88:91], v[44:47], v[64:67]\n"
" v_mul_f32 v55, v98, v55\n"
" v_mul_f32 v52, v52, v54\n"
" v_mfma_f32_16x16x32_bf16 v[68:71], a[92:95], v[44:47], v[68:71]\n"
" s_waitcnt lgkmcnt(3)\n"
" v_mul_f32 v53, v53, v55\n"
" v_cvt_pk_bf16_f32 v113, v52, v53\n"
" v_mfma_f32_16x16x32_bf16 v[60:63], a[96:99], v[16:19], v[60:63]\n"
" ds_read_b128 v[36:39], v8 offset:29696\n"
" v_fmac_f32 v56, v216, v96\n"
" v_mfma_f32_16x16x32_bf16 v[64:67], a[100:103], v[16:19], v[64:67]\n"
" buffer_load_dwordx4 v[204:207], v1, s[16:19], s95 offen offset:3072 sc1\n"
" v_fmac_f32 v57, v217, v96\n"
" v_mfma_f32_16x16x32_bf16 v[68:71], a[104:107], v[16:19], v[68:71]\n"
" v_fmac_f32 v58, v218, v96\n"
" v_fmac_f32 v59, v219, v96\n"
" v_mfma_f32_16x16x32_bf16 v[60:63], a[108:111], v[20:23], v[60:63]\n"
" ds_read_b128 v[40:43], v8 offset:30720\n"
" v_fmac_f32 v56, v228, v97\n"
" v_mfma_f32_16x16x32_bf16 v[64:67], a[112:115], v[20:23], v[64:67]\n"
" v_fmac_f32 v57, v229, v97\n"
" v_fmac_f32 v58, v230, v97\n"
" v_mfma_f32_16x16x32_bf16 v[68:71], a[116:119], v[20:23], v[68:71]\n"
" s_add_u32 s95, s88, 131072\n"
" buffer_load_dwordx4 v[240:243], v1, s[16:19], s95 offen offset:0 sc1\n"
" s_waitcnt lgkmcnt(3)\n"
" v_mfma_f32_16x16x32_bf16 v[60:63], a[120:123], v[24:27], v[60:63]\n"
" ds_read_b128 v[44:47], v8 offset:31744\n"
" v_fmac_f32 v59, v231, v97\n"
" v_mfma_f32_16x16x32_bf16 v[64:67], a[124:127], v[24:27], v[64:67]\n"
" v_max_f32 v56, 0, v56\n"
" v_max_f32 v57, 0, v57\n"
" v_mfma_f32_16x16x32_bf16 v[68:71], a[128:131], v[24:27], v[68:71]\n"
" v_mul_f32 v58, v98, v58\n"
" v_mul_f32 v59, v98, v59\n"
" v_mfma_f32_16x16x32_bf16 v[60:63], a[132:135], v[28:31], v[60:63]\n"
" ds_read_b128 v[16:19], v8 offset:32768\n"
" v_mul_f32 v56, v56, v58\n"
" v_mfma_f32_16x16x32_bf16 v[64:67], a[136:139], v[28:31], v[64:67]\n"
" buffer_load_dwordx4 v[244:247], v1, s[16:19], s95 offen offset:1024 sc1\n"
" v_mul_f32 v57, v57, v59\n"
" v_mfma_f32_16x16x32_bf16 v[68:71], a[140:143], v[28:31], v[68:71]\n"
" s_waitcnt lgkmcnt(3)\n"
" ds_read_b128 v[104:107], v11 offset:1536\n"
" v_cvt_pk_bf16_f32 v116, v56, v57\n"
" v_mfma_f32_16x16x32_bf16 v[60:63], a[144:147], v[32:35], v[60:63]\n"
" ds_read_b128 v[20:23], v8 offset:49152\n"
" v_mfma_f32_16x16x32_bf16 v[64:67], a[148:151], v[32:35], v[64:67]\n"
" ds_read_b128 v[108:111], v11 offset:1792\n"
" v_mfma_f32_16x16x32_bf16 v[68:71], a[152:155], v[32:35], v[68:71]\n"
" v_mfma_f32_16x16x32_bf16 v[60:63], a[156:159], v[36:39], v[60:63]\n"
" ds_read_b128 v[24:27], v8 offset:33792\n"
" v_mfma_f32_16x16x32_bf16 v[64:67], a[160:163], v[36:39], v[64:67]\n"
" buffer_load_dwordx4 v[248:251], v1, s[16:19], s95 offen offset:2048 sc1\n"
" v_mfma_f32_16x16x32_bf16 v[68:71], a[164:167], v[36:39], v[68:71]\n"
" s_waitcnt lgkmcnt(5)\n"
" v_mfma_f32_16x16x32_bf16 v[60:63], a[168:171], v[40:43], v[60:63]\n"
" ds_read_b128 v[28:31], v8 offset:50176\n"
" v_mfma_f32_16x16x32_bf16 v[64:67], a[172:175], v[40:43], v[64:67]\n"
" v_mfma_f32_16x16x32_bf16 v[68:71], a[176:179], v[40:43], v[68:71]\n"
" buffer_load_dwordx4 v[124:127], v1, s[16:19], s95 offen offset:3072 sc1\n"
" v_mfma_f32_16x16x32_bf16 v[60:63], a[180:183], v[44:47], v[60:63]\n"
" ds_read_b128 v[32:35], v8 offset:34816\n"
" v_mfma_f32_16x16x32_bf16 v[64:67], a[184:187], v[44:47], v[64:67]\n"
" v_mfma_f32_16x16x32_bf16 v[68:71], a[188:191], v[44:47], v[68:71]\n"
" s_waitcnt lgkmcnt(4)\n"
" v_mfma_f32_16x16x32_bf16 v[72:75], a[0:3], v[16:19], v[72:75]\n"
" ds_read_b128 v[36:39], v8 offset:51200\n"
" v_mfma_f32_16x16x32_bf16 v[76:79], a[4:7], v[16:19], v[76:79]\n"
" v_mfma_f32_16x16x32_bf16 v[80:83], a[8:11], v[16:19], v[80:83]\n"
" v_mfma_f32_16x16x32_bf16 v[84:87], a[0:3], v[20:23], v[84:87]\n"
" ds_read_b128 v[40:43], v8 offset:35840\n"
" v_fmac_f32 v60, v208, v100\n"
" v_mfma_f32_16x16x32_bf16 v[88:91], a[4:7], v[20:23], v[88:91]\n"
" v_fmac_f32 v61, v209, v100\n"
" v_fmac_f32 v62, v210, v100\n"
" v_mfma_f32_16x16x32_bf16 v[92:95], a[8:11], v[20:23], v[92:95]\n"
" s_waitcnt lgkmcnt(3)\n"
" v_fmac_f32 v63, v211, v100\n"
" v_fmac_f32 v60, v220, v101\n"
" v_mfma_f32_16x16x32_bf16 v[72:75], a[12:15], v[24:27], v[72:75]\n"
" ds_read_b128 v[44:47], v8 offset:52224\n"
" v_fmac_f32 v61, v221, v101\n"
" v_mfma_f32_16x16x32_bf16 v[76:79], a[16:19], v[24:27], v[76:79]\n"
" v_fmac_f32 v62, v222, v101\n"
" v_fmac_f32 v63, v223, v101\n"
" v_mfma_f32_16x16x32_bf16 v[80:83], a[20:23], v[24:27], v[80:83]\n"
" v_max_f32 v60, 0, v60\n"
" v_max_f32 v61, 0, v61\n"
" v_mfma_f32_16x16x32_bf16 v[84:87], a[12:15], v[28:31], v[84:87]\n"
" ds_read_b128 v[16:19], v8 offset:36864\n"
" v_mul_f32 v62, v102, v62\n"
" v_mfma_f32_16x16x32_bf16 v[88:91], a[16:19], v[28:31], v[88:91]\n"
" s_mov_b32 s67, 65536\n"
" s_mov_b32 exec_lo, 0\n"
" s_mov_b32 exec_hi, 0xffff0000\n"
" buffer_load_dword v237, v238, s[16:19], s67 offen sc1\n"
" s_mov_b64 exec, -1\n"
" s_nop 1\n"
" v_mfma_f32_16x16x32_bf16 v[92:95], a[20:23], v[28:31], v[92:95]\n"
" s_waitcnt lgkmcnt(3)\n"
" v_mul_f32 v63, v102, v63\n"
" v_mul_f32 v60, v60, v62\n"
" v_mfma_f32_16x16x32_bf16 v[72:75], a[24:27], v[32:35], v[72:75]\n"
" ds_read_b128 v[20:23], v8 offset:53248\n"
" v_mul_f32 v61, v61, v63\n"
" v_mfma_f32_16x16x32_bf16 v[76:79], a[28:31], v[32:35], v[76:79]\n"
" s_add_u32 s95, s88, 147456\n"
" buffer_load_dwordx4 a[0:3], v1, s[16:19], s95 offen offset:0 sc1\n"
" v_cvt_pk_bf16_f32 v114, v60, v61\n"
" v_mfma_f32_16x16x32_bf16 v[80:83], a[32:35], v[32:35], v[80:83]\n"
" v_fmac_f32 v64, v212, v100\n"
" v_fmac_f32 v65, v213, v100\n"
" v_mfma_f32_16x16x32_bf16 v[84:87], a[24:27], v[36:39], v[84:87]\n"
" ds_read_b128 v[24:27], v8 offset:37888\n"
" v_fmac_f32 v66, v214, v100\n"
" v_mfma_f32_16x16x32_bf16 v[88:91], a[28:31], v[36:39], v[88:91]\n"
" buffer_load_dwordx4 a[4:7], v1, s[16:19], s95 offen offset:1024 sc1\n"
" v_fmac_f32 v67, v215, v100\n"
" v_mfma_f32_16x16x32_bf16 v[92:95], a[32:35], v[36:39], v[92:95]\n"
" s_waitcnt lgkmcnt(3)\n"
" v_fmac_f32 v64, v224, v101\n"
" v_fmac_f32 v65, v225, v101\n"
" v_mfma_f32_16x16x32_bf16 v[72:75], a[36:39], v[40:43], v[72:75]\n"
" ds_read_b128 v[28:31], v8 offset:54272\n"
" v_fmac_f32 v66, v226, v101\n"
" v_mfma_f32_16x16x32_bf16 v[76:79], a[40:43], v[40:43], v[76:79]\n"
" v_fmac_f32 v67, v227, v101\n"
" v_max_f32 v64, 0, v64\n"
" v_mfma_f32_16x16x32_bf16 v[80:83], a[44:47], v[40:43], v[80:83]\n"
" v_max_f32 v65, 0, v65\n"
" v_mul_f32 v66, v102, v66\n"
" v_mfma_f32_16x16x32_bf16 v[84:87], a[36:39], v[44:47], v[84:87]\n"
" ds_read_b128 v[32:35], v8 offset:38912\n"
" v_mul_f32 v67, v102, v67\n"
" v_mfma_f32_16x16x32_bf16 v[88:91], a[40:43], v[44:47], v[88:91]\n"
" buffer_load_dwordx4 a[8:11], v1, s[16:19], s95 offen offset:2048 sc1\n"
" v_mul_f32 v64, v64, v66\n"
" v_mfma_f32_16x16x32_bf16 v[92:95], a[44:47], v[44:47], v[92:95]\n"
" s_waitcnt lgkmcnt(3)\n"
" v_mul_f32 v65, v65, v67\n"
" v_cvt_pk_bf16_f32 v115, v64, v65\n"
" v_mfma_f32_16x16x32_bf16 v[72:75], a[48:51], v[16:19], v[72:75]\n"
" ds_read_b128 v[36:39], v8 offset:55296\n"
" v_mfma_f32_16x16x32_bf16 v[76:79], a[52:55], v[16:19], v[76:79]\n"
" v_permlane32_swap_b32 v112, v114\n"
" v_mfma_f32_16x16x32_bf16 v[80:83], a[56:59], v[16:19], v[80:83]\n"
" buffer_load_dwordx4 a[12:15], v1, s[16:19], s95 offen offset:3072 sc1\n"
" v_mfma_f32_16x16x32_bf16 v[84:87], a[48:51], v[20:23], v[84:87]\n"
" ds_read_b128 v[40:43], v8 offset:39936\n"
" v_mfma_f32_16x16x32_bf16 v[88:91], a[52:55], v[20:23], v[88:91]\n"
" s_add_u32 s51, s94, 176128\n"
" v_permlane32_swap_b32 v113, v115\n"
" v_mfma_f32_16x16x32_bf16 v[92:95], a[56:59], v[20:23], v[92:95]\n"
" s_waitcnt lgkmcnt(3)\n"
" buffer_store_dwordx4 v[112:115], v9, s[16:19], s51 offen sc1\n"
" v_mfma_f32_16x16x32_bf16 v[72:75], a[60:63], v[24:27], v[72:75]\n"
" ds_read_b128 v[44:47], v8 offset:56320\n"
" v_fmac_f32 v68, v216, v100\n"
" v_mfma_f32_16x16x32_bf16 v[76:79], a[64:67], v[24:27], v[76:79]\n"
" v_fmac_f32 v69, v217, v100\n"
" v_fmac_f32 v70, v218, v100\n"
" v_mfma_f32_16x16x32_bf16 v[80:83], a[68:71], v[24:27], v[80:83]\n"
" v_fmac_f32 v71, v219, v100\n"
" v_fmac_f32 v68, v228, v101\n"
" v_mfma_f32_16x16x32_bf16 v[84:87], a[60:63], v[28:31], v[84:87]\n"
" ds_read_b128 v[16:19], v8 offset:40960\n"
" v_fmac_f32 v69, v229, v101\n"
" v_mfma_f32_16x16x32_bf16 v[88:91], a[64:67], v[28:31], v[88:91]\n"
" v_fmac_f32 v70, v230, v101\n"
" v_fmac_f32 v71, v231, v101\n"
" v_mfma_f32_16x16x32_bf16 v[92:95], a[68:71], v[28:31], v[92:95]\n"
" s_waitcnt lgkmcnt(3)\n"
" v_max_f32 v68, 0, v68\n"
" v_max_f32 v69, 0, v69\n"
" v_mfma_f32_16x16x32_bf16 v[72:75], a[72:75], v[32:35], v[72:75]\n"
" ds_read_b128 v[20:23], v8 offset:57344\n"
" v_mul_f32 v70, v102, v70\n"
" v_mfma_f32_16x16x32_bf16 v[76:79], a[76:79], v[32:35], v[76:79]\n"
" v_mul_f32 v71, v102, v71\n"
" v_mul_f32 v68, v68, v70\n"
" v_mfma_f32_16x16x32_bf16 v[80:83], a[80:83], v[32:35], v[80:83]\n"
" v_mul_f32 v69, v69, v71\n"
" v_cvt_pk_bf16_f32 v118, v68, v69\n"
" v_mfma_f32_16x16x32_bf16 v[84:87], a[72:75], v[36:39], v[84:87]\n"
" ds_read_b128 v[24:27], v8 offset:41984\n"
" v_mfma_f32_16x16x32_bf16 v[88:91], a[76:79], v[36:39], v[88:91]\n"
" v_permlane32_swap_b32 v116, v118\n"
" v_mfma_f32_16x16x32_bf16 v[92:95], a[80:83], v[36:39], v[92:95]\n"
" s_waitcnt lgkmcnt(3)\n"
" v_mov_b32 v117, v116\n"
" v_mov_b32 v119, v118\n"
" v_mfma_f32_16x16x32_bf16 v[72:75], a[84:87], v[40:43], v[72:75]\n"
" ds_read_b128 v[28:31], v8 offset:58368\n"
" v_mfma_f32_16x16x32_bf16 v[76:79], a[88:91], v[40:43], v[76:79]\n"
" v_permlane16_swap_b32 v116, v117\n"
" v_mfma_f32_16x16x32_bf16 v[80:83], a[92:95], v[40:43], v[80:83]\n"
" s_add_u32 s51, s56, 176128\n"
" v_permlane16_swap_b32 v118, v119\n"
" v_mfma_f32_16x16x32_bf16 v[84:87], a[84:87], v[44:47], v[84:87]\n"
" s_mov_b64 exec, s[52:53]\n"
" buffer_store_dwordx4 v[116:119], v9, s[16:19], s51 offen sc1\n"
" s_mov_b64 exec, -1\n"
" ds_read_b128 v[32:35], v8 offset:43008\n"
" s_nop 0\n"
" v_mfma_f32_16x16x32_bf16 v[88:91], a[88:91], v[44:47], v[88:91]\n"
" v_mfma_f32_16x16x32_bf16 v[92:95], a[92:95], v[44:47], v[92:95]\n"
" s_waitcnt lgkmcnt(3)\n"
" v_mfma_f32_16x16x32_bf16 v[72:75], a[96:99], v[16:19], v[72:75]\n"
" ds_read_b128 v[36:39], v8 offset:59392\n"
" v_mfma_f32_16x16x32_bf16 v[76:79], a[100:103], v[16:19], v[76:79]\n"
" v_mfma_f32_16x16x32_bf16 v[80:83], a[104:107], v[16:19], v[80:83]\n"
" v_mfma_f32_16x16x32_bf16 v[84:87], a[96:99], v[20:23], v[84:87]\n"
" ds_read_b128 v[40:43], v8 offset:44032\n"
" v_mfma_f32_16x16x32_bf16 v[88:91], a[100:103], v[20:23], v[88:91]\n"
" v_mfma_f32_16x16x32_bf16 v[92:95], a[104:107], v[20:23], v[92:95]\n"
" s_waitcnt lgkmcnt(3)\n"
" v_mfma_f32_16x16x32_bf16 v[72:75], a[108:111], v[24:27], v[72:75]\n"
" ds_read_b128 v[44:47], v8 offset:60416\n"
" v_mfma_f32_16x16x32_bf16 v[76:79], a[112:115], v[24:27], v[76:79]\n"
" v_mfma_f32_16x16x32_bf16 v[80:83], a[116:119], v[24:27], v[80:83]\n"
" v_mfma_f32_16x16x32_bf16 v[84:87], a[108:111], v[28:31], v[84:87]\n"
" ds_read_b128 v[16:19], v8 offset:45056\n"
" v_mfma_f32_16x16x32_bf16 v[88:91], a[112:115], v[28:31], v[88:91]\n"
" v_mfma_f32_16x16x32_bf16 v[92:95], a[116:119], v[28:31], v[92:95]\n"
" s_waitcnt lgkmcnt(3)\n"
" v_mfma_f32_16x16x32_bf16 v[72:75], a[120:123], v[32:35], v[72:75]\n"
" ds_read_b128 v[20:23], v8 offset:61440\n"
" v_mfma_f32_16x16x32_bf16 v[76:79], a[124:127], v[32:35], v[76:79]\n"
" v_mfma_f32_16x16x32_bf16 v[80:83], a[128:131], v[32:35], v[80:83]\n"
" v_mfma_f32_16x16x32_bf16 v[84:87], a[120:123], v[36:39], v[84:87]\n"
" ds_read_b128 v[24:27], v8 offset:46080\n"
" v_mfma_f32_16x16x32_bf16 v[88:91], a[124:127], v[36:39], v[88:91]\n"
" v_mfma_f32_16x16x32_bf16 v[92:95], a[128:131], v[36:39], v[92:95]\n"
" s_waitcnt lgkmcnt(3)\n"
" v_mfma_f32_16x16x32_bf16 v[72:75], a[132:135], v[40:43], v[72:75]\n"
" ds_read_b128 v[28:31], v8 offset:62464\n"
" v_mfma_f32_16x16x32_bf16 v[76:79], a[136:139], v[40:43], v[76:79]\n"
".Lf3_chk:\n"
" s_waitcnt vmcnt(6)\n"
" v_cmp_ne_u32 vcc, s48, v237\n"
" s_nop 0\n"
" s_cbranch_vccnz .Lf3_slow\n"
".Lf3_ok:\n"
" s_add_u32 s68, s90, 0\n"
" buffer_load_dwordx4 a[16:19], v1, s[16:19], s68 offen sc1\n"
" v_mfma_f32_16x16x32_bf16 v[80:83], a[140:143], v[40:43], v[80:83]\n"
" v_mfma_f32_16x16x32_bf16 v[84:87], a[132:135], v[44:47], v[84:87]\n"
" ds_read_b128 v[32:35], v8 offset:47104\n"
" v_mfma_f32_16x16x32_bf16 v[88:91], a[136:139], v[44:47], v[88:91]\n"
" s_add_u32 s68, s90, 4096\n"
" buffer_load_dwordx4 a[20:23], v1, s[16:19], s68 offen sc1\n"
" v_mfma_f32_16x16x32_bf16 v[92:95], a[140:143], v[44:47], v[92:95]\n"
" s_waitcnt lgkmcnt(3)\n"
" v_mfma_f32_16x16x32_bf16 v[72:75], a[144:147], v[16:19], v[72:75]\n"
" ds_read_b128 v[36:39], v8 offset:63488\n"
" v_mfma_f32_16x16x32_bf16 v[76:79], a[148:151], v[16:19], v[76:79]\n"
" v_mfma_f32_16x16x32_bf16 v[80:83], a[152:155], v[16:19], v[80:83]\n"
" s_add_u32 s68, s90, 8192\n"
" buffer_load_dwordx4 a[24:27], v1, s[16:19], s68 offen sc1\n"
" v_mfma_f32_16x16x32_bf16 v[84:87], a[144:147], v[20:23], v[84:87]\n"
" ds_read_b128 v[40:43], v8 offset:48128\n"
" v_mfma_f32_16x16x32_bf16 v[88:91], a[148:151], v[20:23], v[88:91]\n"
" v_mfma_f32_16x16x32_bf16 v[92:95], a[152:155], v[20:23], v[92:95]\n"
" s_waitcnt lgkmcnt(3)\n"
" v_mfma_f32_16x16x32_bf16 v[72:75], a[156:159], v[24:27], v[72:75]\n"
" ds_read_b128 v[44:47], v8 offset:64512\n"
" v_mfma_f32_16x16x32_bf16 v[76:79], a[160:163], v[24:27], v[76:79]\n"
" s_add_u32 s68, s90, 12288\n"
" buffer_load_dwordx4 a[28:31], v1, s[16:19], s68 offen sc1\n"
" v_mfma_f32_16x16x32_bf16 v[80:83], a[164:167], v[24:27], v[80:83]\n"
" v_mfma_f32_16x16x32_bf16 v[84:87], a[156:159], v[28:31], v[84:87]\n"
" v_mfma_f32_16x16x32_bf16 v[88:91], a[160:163], v[28:31], v[88:91]\n"
" s_add_u32 s68, s90, 16384\n"
" buffer_load_dwordx4 a[32:35], v1, s[16:19], s68 offen sc1\n"
" v_mfma_f32_16x16x32_bf16 v[92:95], a[164:167], v[28:31], v[92:95]\n"
" s_waitcnt lgkmcnt(2)\n"
" v_mfma_f32_16x16x32_bf16 v[72:75], a[168:171], v[32:35], v[72:75]\n"
" v_mfma_f32_16x16x32_bf16 v[76:79], a[172:175], v[32:35], v[76:79]\n"
" v_mfma_f32_16x16x32_bf16 v[80:83], a[176:179], v[32:35], v[80:83]\n"
" s_add_u32 s68, s90, 20480\n"
" buffer_load_dwordx4 a[36:39], v1, s[16:19], s68 offen sc1\n"
" v_mfma_f32_16x16x32_bf16 v[84:87], a[168:171], v[36:39], v[84:87]\n"
" v_mfma_f32_16x16x32_bf16 v[88:91], a[172:175], v[36:39], v[88:91]\n"
" v_mfma_f32_16x16x32_bf16 v[92:95], a[176:179], v[36:39], v[92:95]\n"
" s_waitcnt lgkmcnt(0)\n"
" v_mfma_f32_16x16x32_bf16 v[72:75], a[180:183], v[40:43], v[72:75]\n"
" v_mfma_f32_16x16x32_bf16 v[76:79], a[184:187], v[40:43], v[76:79]\n"
" s_add_u32 s68, s90, 24576\n"
" buffer_load_dwordx4 a[40:43], v1, s[16:19], s68 offen sc1\n"
" v_mfma_f32_16x16x32_bf16 v[80:83], a[188:191], v[40:43], v[80:83]\n"
" v_mfma_f32_16x16x32_bf16 v[84:87], a[180:183], v[44:47], v[84:87]\n"
" v_mfma_f32_16x16x32_bf16 v[88:91], a[184:187], v[44:47], v[88:91]\n"
" v_mfma_f32_16x16x32_bf16 v[92:95], a[188:191], v[44:47], v[92:95]\n"
" s_waitcnt vmcnt(7)\n"
" s_barrier\n"
" s_cmp_eq_u32 s3, 3\n"
" s_cbranch_scc0 .LL_f5\n"
" s_lshl_b32 s51, s2, 7\n"
" s_add_u32 s51, s51, 16711680\n"
" v_mov_b32 v232, s48\n"
" v_lshlrev_b32 v233, 2, v0\n"
" s_mov_b32 exec_hi, 0\n"
" buffer_store_dword v232, v233, s[16:19], s51 offen sc1\n"
" s_mov_b64 exec, -1\n"
".LL_f5:\n"
" s_add_u32 s68, s90, 28672\n"
" buffer_load_dwordx4 a[44:47], v1, s[16:19], s68 offen sc1\n"
" v_max_f32 v72, 0, v72\n"
" v_max_f32 v73, 0, v73\n"
" v_pk_mul_f32 v[74:75], v[74:75], v[106:107] op_sel_hi:[1,0]\n"
" v_pk_mul_f32 v[72:73], v[72:73], v[74:75]\n"
" v_cvt_pk_bf16_f32 v112, v72, v73\n"
" v_max_f32 v84, 0, v84\n"
" v_max_f32 v85, 0, v85\n"
" v_pk_mul_f32 v[86:87], v[86:87], v[110:111] op_sel_hi:[1,0]\n"
" v_pk_mul_f32 v[84:85], v[84:85], v[86:87]\n"
" v_cvt_pk_bf16_f32 v114, v84, v85\n"
" v_max_f32 v76, 0, v76\n"
" v_max_f32 v77, 0, v77\n"
" v_pk_mul_f32 v[78:79], v[78:79], v[106:107] op_sel_hi:[1,0]\n"
" v_pk_mul_f32 v[76:77], v[76:77], v[78:79]\n"
" v_cvt_pk_bf16_f32 v113, v76, v77\n"
" v_max_f32 v88, 0, v88\n"
" v_max_f32 v89, 0, v89\n"
" v_pk_mul_f32 v[90:91], v[90:91], v[110:111] op_sel_hi:[1,0]\n"
" v_pk_mul_f32 v[88:89], v[88:89], v[90:91]\n"
" v_cvt_pk_bf16_f32 v115, v88, v89\n"
" v_permlane32_swap_b32 v112, v114\n"
" s_nop 0\n"
" v_permlane32_swap_b32 v113, v115\n"
" s_add_u32 s51, s94, 264192\n"
" buffer_store_dwordx4 v[112:115], v9, s[16:19], s51 offen sc1\n"
" v_max_f32 v80, 0, v80\n"
" v_max_f32 v81, 0, v81\n"
" v_pk_mul_f32 v[82:83], v[82:83], v[106:107] op_sel_hi:[1,0]\n"
" v_pk_mul_f32 v[80:81], v[80:81], v[82:83]\n"
" v_cvt_pk_bf16_f32 v116, v80, v81\n"
" v_max_f32 v92, 0, v92\n"
" v_max_f32 v93, 0, v93\n"
" v_pk_mul_f32 v[94:95], v[94:95], v[110:111] op_sel_hi:[1,0]\n"
" v_pk_mul_f32 v[92:93], v[92:93], v[94:95]\n"
" v_cvt_pk_bf16_f32 v118, v92, v93\n"
" s_nop 1\n"
" v_permlane32_swap_b32 v116, v118\n"
" v_mov_b32 v117, v116\n"
" v_mov_b32 v119, v118\n"
" s_nop 0\n"
" v_permlane16_swap_b32 v116, v117\n"
" v_permlane16_swap_b32 v118, v119\n"
" s_add_u32 s51, s56, 264192\n"
" s_mov_b64 exec, s[52:53]\n"
" buffer_store_dwordx4 v[116:119], v9, s[16:19], s51 offen sc1\n"
" s_mov_b64 exec, -1\n"
" s_add_u32 s68, s90, 32768\n"
" buffer_load_dwordx4 a[48:51], v1, s[16:19], s68 offen sc1\n"
" s_waitcnt vmcnt(10)\n"
" v_mfma_f32_16x16x32_bf16 v[16:19], a[208:211], a[16:19], 0\n"
" v_subrev_u32 v12, 131072, v12\n"
" v_subrev_u32 v13, 131072, v13\n"
" v_mfma_f32_16x16x32_bf16 v[20:23], a[212:215], a[16:19], 0\n"
" v_subrev_u32 v14, 131072, v14\n"
" v_subrev_u32 v15, 131072, v15\n"
" v_mfma_f32_16x16x32_bf16 v[24:27], a[216:219], a[16:19], 0\n"
" v_lshl_add_u32 v4, s3, 2, v2\n"
" v_xor_b32 v4, v4, v3\n"
" v_mfma_f32_16x16x32_bf16 v[28:31], a[220:223], a[16:19], 0\n"
" s_add_u32 s68, s90, 36864\n"
" buffer_load_dwordx4 a[52:55], v1, s[16:19], s68 offen sc1\n"
" v_lshlrev_b32 v4, 4, v4\n"
" s_waitcnt vmcnt(10)\n"
" v_mfma_f32_16x16x32_bf16 v[16:19], a[224:227], a[20:23], v[16:19]\n"
" v_lshl_add_u32 v9, v3, 8, v4\n"
" v_add_u32 v10, 131072, v9\n"
" v_mfma_f32_16x16x32_bf16 v[20:23], a[228:231], a[20:23], v[20:23]\n"
" v_add_u32 v8, 4294967040, v11\n"
" ds_read_b128 v[112:115], v8\n"
" v_mfma_f32_16x16x32_bf16 v[24:27], a[232:235], a[20:23], v[24:27]\n"
" ds_read_b128 v[96:99], v10 offset:0\n"
" v_mfma_f32_16x16x32_bf16 v[28:31], a[236:239], a[20:23], v[28:31]\n"
" s_add_u32 s68, s90, 40960\n"
" s_add_u32 s68, s68, s89\n"
" buffer_load_dwordx4 a[56:59], v1, s[16:19], s68 offen sc1\n"
" s_waitcnt vmcnt(10)\n"
" v_mfma_f32_16x16x32_bf16 v[16:19], a[240:243], a[24:27], v[16:19]\n"
" ds_read_b128 v[100:103], v10 offset:4096\n"
" v_mfma_f32_16x16x32_bf16 v[20:23], a[244:247], a[24:27], v[20:23]\n"
" ds_read_b128 v[104:107], v10 offset:8192\n"
" v_mfma_f32_16x16x32_bf16 v[24:27], a[248:251], a[24:27], v[24:27]\n"
" ds_read_b128 v[108:111], v10 offset:12288\n"
" v_lshlrev_b32 v11, 3, v3\n"
" v_mfma_f32_16x16x32_bf16 v[28:31], a[252:255], a[24:27], v[28:31]\n"
" s_add_u32 s68, s90, 88064\n"
" buffer_load_dwordx4 a[60:63], v1, s[16:19], s68 offen sc1\n"
" v_lshl_add_u32 v11, v2, 10, v11\n"
" s_waitcnt vmcnt(10)\n"
" v_mfma_f32_16x16x32_bf16 v[16:19], v[128:131], a[28:31], v[16:19]\n"
" s_lshl4_add_u32 s65, s57, s3\n"
" s_lshl_b32 s65, s65, 5\n"
" s_add_u32 s65, s65, s58\n"
" v_mfma_f32_16x16x32_bf16 v[20:23], v[132:135], a[28:31], v[20:23]\n"
" s_lshl_b32 s87, s65, 7\n"
" v_mfma_f32_16x16x32_bf16 v[24:27], v[136:139], a[28:31], v[24:27]\n"
" v_mfma_f32_16x16x32_bf16 v[28:31], v[140:143], a[28:31], v[28:31]\n"
" s_add_u32 s68, s90, 92160\n"
" buffer_load_dwordx4 a[64:67], v1, s[16:19], s68 offen sc1\n"
" s_waitcnt vmcnt(10)\n"
" v_mfma_f32_16x16x32_bf16 v[16:19], v[144:147], a[32:35], v[16:19]\n"
" v_mfma_f32_16x16x32_bf16 v[20:23], v[148:151], a[32:35], v[20:23]\n"
" v_mfma_f32_16x16x32_bf16 v[24:27], v[152:155], a[32:35], v[24:27]\n"
" v_mfma_f32_16x16x32_bf16 v[28:31], v[156:159], a[32:35], v[28:31]\n"
" s_add_u32 s68, s90, 96256\n"
" buffer_load_dwordx4 a[68:71], v1, s[16:19], s68 offen sc1\n"
" s_waitcnt vmcnt(10)\n"
" v_mfma_f32_16x16x32_bf16 v[16:19], v[160:163], a[36:39], v[16:19]\n"
" v_mfma_f32_16x16x32_bf16 v[20:23], v[164:167], a[36:39], v[20:23]\n"
" v_mfma_f32_16x16x32_bf16 v[24:27], v[168:171], a[36:39], v[24:27]\n"
" s_mov_b32 s67, 16285696\n"
" s_mov_b32 exec_lo, 0\n"
" s_mov_b32 exec_hi, 0xffff0000\n"
" buffer_load_dword v237, v238, s[16:19], s67 offen sc1\n"
" s_mov_b64 exec, -1\n"
" s_nop 1\n"
" v_mfma_f32_16x16x32_bf16 v[28:31], v[172:175], a[36:39], v[28:31]\n"
" s_add_u32 s68, s90, 100352\n"
" buffer_load_dwordx4 a[72:75], v1, s[16:19], s68 offen sc1\n"
" s_waitcnt vmcnt(11)\n"
" v_mfma_f32_16x16x32_bf16 v[16:19], v[176:179], a[40:43], v[16:19]\n"
" v_mfma_f32_16x16x32_bf16 v[20:23], v[180:183], a[40:43], v[20:23]\n"
" v_mfma_f32_16x16x32_bf16 v[24:27], v[184:187], a[40:43], v[24:27]\n"
" v_mfma_f32_16x16x32_bf16 v[28:31], v[188:191], a[40:43], v[28:31]\n"
" s_add_u32 s68, s90, 104448\n"
" buffer_load_dwordx4 a[76:79], v1, s[16:19], s68 offen sc1\n"
" s_waitcnt vmcnt(11)\n"
" v_mfma_f32_16x16x32_bf16 v[16:19], v[192:195], a[44:47], v[16:19]\n"
" v_mfma_f32_16x16x32_bf16 v[20:23], v[196:199], a[44:47], v[20:23]\n"
" v_mfma_f32_16x16x32_bf16 v[24:27], v[200:203], a[44:47], v[24:27]\n"
" v_mfma_f32_16x16x32_bf16 v[28:31], v[204:207], a[44:47], v[28:31]\n"
" s_add_u32 s68, s90, 108544\n"
" buffer_load_dwordx4 a[80:83], v1, s[16:19], s68 offen sc1\n"
" s_waitcnt vmcnt(9)\n"
" v_mfma_f32_16x16x32_bf16 v[16:19], v[240:243], a[48:51], v[16:19]\n"
" v_mfma_f32_16x16x32_bf16 v[20:23], v[244:247], a[48:51], v[20:23]\n"
" v_mfma_f32_16x16x32_bf16 v[24:27], v[248:251], a[48:51], v[24:27]\n"
" v_mfma_f32_16x16x32_bf16 v[28:31], v[124:127], a[48:51], v[28:31]\n"
" s_add_u32 s68, s90, 112640\n"
" buffer_load_dwordx4 a[84:87], v1, s[16:19], s68 offen sc1\n"
" s_waitcnt vmcnt(9)\n"
" v_mfma_f32_16x16x32_bf16 v[16:19], a[0:3], a[52:55], v[16:19]\n"
" v_mfma_f32_16x16x32_bf16 v[20:23], a[4:7], a[52:55], v[20:23]\n"
" v_mfma_f32_16x16x32_bf16 v[24:27], a[8:11], a[52:55], v[24:27]\n"
" v_mfma_f32_16x16x32_bf16 v[28:31], a[12:15], a[52:55], v[28:31]\n"
" s_add_u32 s68, s90, 116736\n"
" buffer_load_dwordx4 a[88:91], v1, s[16:19], s68 offen sc1\n"
" s_waitcnt vmcnt(9)\n"
" v_mfma_f32_16x16x32_bf16 v[16:19], a[192:195], a[56:59], v[16:19]\n"
" s_barrier\n"
" s_cmp_eq_u32 s3, 3\n"
" s_cbranch_scc0 .LL_f6\n"
" s_lshl_b32 s51, s2, 7\n"
" s_add_u32 s51, s51, 16744448\n"
" v_mov_b32 v232, s48\n"
" v_lshlrev_b32 v233, 2, v0\n"
" s_mov_b32 exec_hi, 0\n"
" buffer_store_dword v232, v233, s[16:19], s51 offen sc1\n"
" s_mov_b64 exec, -1\n"
".LL_f6:\n"
" s_nop 1\n"
" v_mfma_f32_16x16x32_bf16 v[20:23], a[196:199], a[56:59], v[20:23]\n"
" v_mfma_f32_16x16x32_bf16 v[24:27], a[200:203], a[56:59], v[24:27]\n"
" v_mfma_f32_16x16x32_bf16 v[28:31], a[204:207], a[56:59], v[28:31]\n"
" s_add_u32 s68, s90, 120832\n"
" buffer_load_dwordx4 a[92:95], v1, s[16:19], s68 offen sc1\n"
" s_waitcnt vmcnt(9)\n"
" v_mfma_f32_16x16x32_bf16 v[32:35], a[208:211], a[60:63], 0\n"
" v_mfma_f32_16x16x32_bf16 v[36:39], a[212:215], a[60:63], 0\n"
" v_mfma_f32_16x16x32_bf16 v[40:43], a[216:219], a[60:63], 0\n"
" v_mfma_f32_16x16x32_bf16 v[44:47], a[220:223], a[60:63], 0\n"
" s_add_u32 s68, s90, 124928\n"
" buffer_load_dwordx4 a[96:99], v1, s[16:19], s68 offen sc1\n"
" s_waitcnt vmcnt(9)\n"
" v_mfma_f32_16x16x32_bf16 v[32:35], a[224:227], a[64:67], v[32:35]\n"
" v_mfma_f32_16x16x32_bf16 v[36:39], a[228:231], a[64:67], v[36:39]\n"
" v_mfma_f32_16x16x32_bf16 v[40:43], a[232:235], a[64:67], v[40:43]\n"
" ds_write_b128 v12, v[16:19] offset:0\n"
" v_mfma_f32_16x16x32_bf16 v[44:47], a[236:239], a[64:67], v[44:47]\n"
" s_add_u32 s68, s90, 129024\n"
" s_add_u32 s68, s68, s89\n"
" buffer_load_dwordx4 a[100:103], v1, s[16:19], s68 offen sc1\n"
" s_waitcnt vmcnt(9)\n"
" v_mfma_f32_16x16x32_bf16 v[32:35], a[240:243], a[68:71], v[32:35]\n"
" v_mfma_f32_16x16x32_bf16 v[36:39], a[244:247], a[68:71], v[36:39]\n"
" v_mfma_f32_16x16x32_bf16 v[40:43], a[248:251], a[68:71], v[40:43]\n"
" ds_write_b128 v13, v[20:23] offset:1024\n"
".Lf5_chk:\n"
" s_waitcnt vmcnt(8)\n"
" v_cmp_ne_u32 vcc, s48, v237\n"
" s_nop 0\n"
" s_cbranch_vccnz .Lf5_slow\n"
".Lf5_ok:\n"
" s_add_u32 s68, s90, 176128\n"
" buffer_load_dwordx4 a[104:107], v1, s[16:19], s68 offen sc1\n"
" v_mfma_f32_16x16x32_bf16 v[44:47], a[252:255], a[68:71], v[44:47]\n"
" s_waitcnt vmcnt(8)\n"
" v_mfma_f32_16x16x32_bf16 v[32:35], v[128:131], a[72:75], v[32:35]\n"
" v_mfma_f32_16x16x32_bf16 v[36:39], v[132:135], a[72:75], v[36:39]\n"
" v_mfma_f32_16x16x32_bf16 v[40:43], v[136:139], a[72:75], v[40:43]\n"
" s_add_u32 s68, s90, 180224\n"
" ds_write_b128 v14, v[24:27] offset:2048\n"
" buffer_load_dwordx4 a[108:111], v1, s[16:19], s68 offen sc1\n"
" v_mfma_f32_16x16x32_bf16 v[44:47], v[140:143], a[72:75], v[44:47]\n"
" s_waitcnt vmcnt(8)\n"
" v_mfma_f32_16x16x32_bf16 v[32:35], v[144:147], a[76:79], v[32:35]\n"
" v_mfma_f32_16x16x32_bf16 v[36:39], v[148:151], a[76:79], v[36:39]\n"
" v_mfma_f32_16x16x32_bf16 v[40:43], v[152:155], a[76:79], v[40:43]\n"
" s_add_u32 s68, s90, 184320\n"
" ds_write_b128 v15, v[28:31] offset:3072\n"
" buffer_load_dwordx4 a[112:115], v1, s[16:19], s68 offen sc1\n"
" v_mfma_f32_16x16x32_bf16 v[44:47], v[156:159], a[76:79], v[44:47]\n"
" s_waitcnt vmcnt(8)\n"
" v_mfma_f32_16x16x32_bf16 v[32:35], v[160:163], a[80:83], v[32:35]\n"
" v_mfma_f32_16x16x32_bf16 v[36:39], v[164:167], a[80:83], v[36:39]\n"
" v_mfma_f32_16x16x32_bf16 v[40:43], v[168:171], a[80:83], v[40:43]\n"
" s_add_u32 s68, s90, 188416\n"
" buffer_load_dwordx4 a[116:119], v1, s[16:19], s68 offen sc1\n"
" v_mfma_f32_16x16x32_bf16 v[44:47], v[172:175], a[80:83], v[44:47]\n"
" s_waitcnt vmcnt(8)\n"
" v_mfma_f32_16x16x32_bf16 v[32:35], v[176:179], a[84:87], v[32:35]\n"
" v_mfma_f32_16x16x32_bf16 v[36:39], v[180:183], a[84:87], v[36:39]\n"
" v_mfma_f32_16x16x32_bf16 v[40:43], v[184:187], a[84:87], v[40:43]\n"
" s_add_u32 s68, s90, 192512\n"
" buffer_load_dwordx4 a[120:123], v1, s[16:19], s68 offen sc1\n"
" v_mfma_f32_16x16x32_bf16 v[44:47], v[188:191], a[84:87], v[44:47]\n"
" s_waitcnt vmcnt(8)\n"
" v_mfma_f32_16x16x32_bf16 v[32:35], v[192:195], a[88:91], v[32:35]\n"
" v_mfma_f32_16x16x32_bf16 v[36:39], v[196:199], a[88:91], v[36:39]\n"
" v_mfma_f32_16x16x32_bf16 v[40:43], v[200:203], a[88:91], v[40:43]\n"
" s_add_u32 s68, s90, 196608\n"
" buffer_load_dwordx4 a[124:127], v1, s[16:19], s68 offen sc1\n"
" v_mfma_f32_16x16x32_bf16 v[44:47], v[204:207], a[88:91], v[44:47]\n"
" s_waitcnt vmcnt(8)\n"
" v_mfma_f32_16x16x32_bf16 v[32:35], v[240:243], a[92:95], v[32:35]\n"
" v_mfma_f32_16x16x32_bf16 v[36:39], v[244:247], a[92:95], v[36:39]\n"
" v_mfma_f32_16x16x32_bf16 v[40:43], v[248:251], a[92:95], v[40:43]\n"
" s_add_u32 s68, s90, 200704\n"
" buffer_load_dwordx4 a[128:131], v1, s[16:19], s68 offen sc1\n"
" v_mfma_f32_16x16x32_bf16 v[44:47], v[124:127], a[92:95], v[44:47]\n"
" s_waitcnt vmcnt(8)\n"
" v_mfma_f32_16x16x32_bf16 v[32:35], a[0:3], a[96:99], v[32:35]\n"
" v_mfma_f32_16x16x32_bf16 v[36:39], a[4:7], a[96:99], v[36:39]\n"
" v_mfma_f32_16x16x32_bf16 v[40:43], a[8:11], a[96:99], v[40:43]\n"
" s_add_u32 s68, s90, 204800\n"
" buffer_load_dwordx4 a[132:135], v1, s[16:19], s68 offen sc1\n"
" v_mfma_f32_16x16x32_bf16 v[44:47], a[12:15], a[96:99], v[44:47]\n"
" s_waitcnt vmcnt(8)\n"
" v_mfma_f32_16x16x32_bf16 v[32:35], a[192:195], a[100:103], v[32:35]\n"
" v_mfma_f32_16x16x32_bf16 v[36:39], a[196:199], a[100:103], v[36:39]\n"
" v_mfma_f32_16x16x32_bf16 v[40:43], a[200:203], a[100:103], v[40:43]\n"
" v_mfma_f32_16x16x32_bf16 v[44:47], a[204:207], a[100:103], v[44:47]\n"
" s_mov_b32 s67, 16318464\n"
" s_mov_b32 exec_lo, 0\n"
" s_mov_b32 exec_hi, 0xffff0000\n"
" buffer_load_dword v237, v238, s[16:19], s67 offen sc1\n"
" s_mov_b64 exec, -1\n"
" s_add_u32 s68, s90, 208896\n"
" buffer_load_dwordx4 a[136:139], v1, s[16:19], s68 offen sc1\n"
" s_waitcnt vmcnt(9)\n"
" v_mfma_f32_16x16x32_bf16 v[48:51], a[208:211], a[104:107], 0\n"
" v_mfma_f32_16x16x32_bf16 v[52:55], a[212:215], a[104:107], 0\n"
" v_mfma_f32_16x16x32_bf16 v[56:59], a[216:219], a[104:107], 0\n"
" v_mfma_f32_16x16x32_bf16 v[60:63], a[220:223], a[104:107], 0\n"
" s_add_u32 s68, s90, 212992\n"
" ds_write_b128 v12, v[32:35] offset:16384\n"
" buffer_load_dwordx4 a[140:143], v1, s[16:19], s68 offen sc1\n"
" s_waitcnt vmcnt(9)\n"
" v_mfma_f32_16x16x32_bf16 v[48:51], a[224:227], a[108:111], v[48:51]\n"
" v_mfma_f32_16x16x32_bf16 v[52:55], a[228:231], a[108:111], v[52:55]\n"
" v_mfma_f32_16x16x32_bf16 v[56:59], a[232:235], a[108:111], v[56:59]\n"
" ds_write_b128 v13, v[36:39] offset:17408\n"
" v_mfma_f32_16x16x32_bf16 v[60:63], a[236:239], a[108:111], v[60:63]\n"
" s_add_u32 s68, s90, 217088\n"
" s_add_u32 s68, s68, s89\n"
" buffer_load_dwordx4 a[144:147], v1, s[16:19], s68 offen sc1\n"
" s_waitcnt vmcnt(9)\n"
" v_mfma_f32_16x16x32_bf16 v[48:51], a[240:243], a[112:115], v[48:51]\n"
" v_mfma_f32_16x16x32_bf16 v[52:55], a[244:247], a[112:115], v[52:55]\n"
" ds_write_b128 v14, v[40:43] offset:18432\n"
" v_mfma_f32_16x16x32_bf16 v[56:59], a[248:251], a[112:115], v[56:59]\n"
" v_mfma_f32_16x16x32_bf16 v[60:63], a[252:255], a[112:115], v[60:63]\n"
" s_waitcnt vmcnt(8)\n"
" v_mfma_f32_16x16x32_bf16 v[48:51], v[128:131], a[116:119], v[48:51]\n"
" ds_write_b128 v15, v[44:47] offset:19456\n"
" v_mfma_f32_16x16x32_bf16 v[52:55], v[132:135], a[116:119], v[52:55]\n"
" v_mfma_f32_16x16x32_bf16 v[56:59], v[136:139], a[116:119], v[56:59]\n"
" v_mfma_f32_16x16x32_bf16 v[60:63], v[140:143], a[116:119], v[60:63]\n"
" s_waitcnt lgkmcnt(0)\n"
" s_barrier\n"
" s_waitcnt vmcnt(7)\n"
" v_mfma_f32_16x16x32_bf16 v[48:51], v[144:147], a[120:123], v[48:51]\n"
" ds_read_b128 v[208:211], v9 offset:0\n"
" v_mfma_f32_16x16x32_bf16 v[52:55], v[148:151], a[120:123], v[52:55]\n"
" ds_read_b128 v[212:215], v9 offset:4096\n"
" v_mfma_f32_16x16x32_bf16 v[56:59], v[152:155], a[120:123], v[56:59]\n"
" ds_read_b128 v[216:219], v9 offset:8192\n"
" v_mfma_f32_16x16x32_bf16 v[60:63], v[156:159], a[120:123], v[60:63]\n"
" ds_read_b128 v[220:223], v9 offset:12288\n"
" s_waitcnt vmcnt(6)\n"
" v_mfma_f32_16x16x32_bf16 v[48:51], v[160:163], a[124:127], v[48:51]\n"
" ds_read_b128 v[80:83], v9 offset:16384\n"
" v_mfma_f32_16x16x32_bf16 v[52:55], v[164:167], a[124:127], v[52:55]\n"
" ds_read_b128 v[84:87], v9 offset:20480\n"
" v_mfma_f32_16x16x32_bf16 v[56:59], v[168:171], a[124:127], v[56:59]\n"
" ds_read_b128 v[88:91], v9 offset:24576\n"
" v_mfma_f32_16x16x32_bf16 v[60:63], v[172:175], a[124:127], v[60:63]\n"
" ds_read_b128 v[92:95], v9 offset:28672\n"
" s_waitcnt vmcnt(5)\n"
" v_mfma_f32_16x16x32_bf16 v[48:51], v[176:179], a[128:131], v[48:51]\n"
" v_mfma_f32_16x16x32_bf16 v[52:55], v[180:183], a[128:131], v[52:55]\n"
" s_waitcnt lgkmcnt(6)\n"
" v_add_f32 v208, v208, v212\n"
" v_add_f32 v209, v209, v213\n"
" v_mfma_f32_16x16x32_bf16 v[56:59], v[184:187], a[128:131], v[56:59]\n"
" v_add_f32 v210, v210, v214\n"
" v_add_f32 v211, v211, v215\n"
" v_mfma_f32_16x16x32_bf16 v[60:63], v[188:191], a[128:131], v[60:63]\n"
" s_waitcnt lgkmcnt(4)\n"
" v_add_f32 v216, v216, v220\n"
" v_add_f32 v217, v217, v221\n"
" s_waitcnt vmcnt(4)\n"
" v_mfma_f32_16x16x32_bf16 v[48:51], v[192:195], a[132:135], v[48:51]\n"
".Lf6_chk:\n"
" s_waitcnt vmcnt(3)\n"
" v_cmp_ne_u32 vcc, s48, v237\n"
" s_nop 0\n"
" s_cbranch_vccnz .Lf6_slow\n"
".Lf6_ok:\n"
" s_add_u32 s68, s90, 264192\n"
" buffer_load_dwordx4 a[148:151], v1, s[16:19], s68 offen sc1\n"
" v_mfma_f32_16x16x32_bf16 v[52:55], v[196:199], a[132:135], v[52:55]\n"
" v_add_f32 v218, v218, v222\n"
" v_add_f32 v219, v219, v223\n"
" v_mfma_f32_16x16x32_bf16 v[56:59], v[200:203], a[132:135], v[56:59]\n"
" s_add_u32 s68, s90, 268288\n"
" buffer_load_dwordx4 a[152:155], v1, s[16:19], s68 offen sc1\n"
" v_add_f32 v208, v208, v216\n"
" v_mfma_f32_16x16x32_bf16 v[60:63], v[204:207], a[132:135], v[60:63]\n"
" v_add_f32 v209, v209, v217\n"
" v_add_f32 v210, v210, v218\n"
" s_waitcnt vmcnt(4)\n"
" v_mfma_f32_16x16x32_bf16 v[48:51], v[240:243], a[136:139], v[48:51]\n"
" s_add_u32 s68, s90, 272384\n"
" buffer_load_dwordx4 a[156:159], v1, s[16:19], s68 offen sc1\n"
" v_add_f32 v211, v211, v219\n"
" v_mfma_f32_16x16x32_bf16 v[52:55], v[244:247], a[136:139], v[52:55]\n"
" v_fma_f32 v208, v208, v112, v96\n"
" v_fma_f32 v209, v209, v113, v97\n"
" v_mfma_f32_16x16x32_bf16 v[56:59], v[248:251], a[136:139], v[56:59]\n"
" s_add_u32 s68, s90, 276480\n"
" buffer_load_dwordx4 a[160:163], v1, s[16:19], s68 offen sc1\n"
" v_fma_f32 v210, v210, v114, v98\n"
" v_mfma_f32_16x16x32_bf16 v[60:63], v[124:127], a[136:139], v[60:63]\n"
" v_fma_f32 v211, v211, v115, v99\n"
" v_cvt_pk_bf16_f32 v116, v208, v209\n"
" s_waitcnt vmcnt(5)\n"
" v_mfma_f32_16x16x32_bf16 v[48:51], a[0:3], a[140:143], v[48:51]\n"
" s_add_u32 s68, s90, 280576\n"
" buffer_load_dwordx4 a[164:167], v1, s[16:19], s68 offen sc1\n"
" v_cvt_pk_bf16_f32 v117, v210, v211\n"
" v_mfma_f32_16x16x32_bf16 v[52:55], a[4:7], a[140:143], v[52:55]\n"
" s_waitcnt lgkmcnt(2)\n"
" v_add_f32 v80, v80, v84\n"
" v_add_f32 v81, v81, v85\n"
" v_mfma_f32_16x16x32_bf16 v[56:59], a[8:11], a[140:143], v[56:59]\n"
" s_add_u32 s68, s90, 284672\n"
" buffer_load_dwordx4 a[168:171], v1, s[16:19], s68 offen sc1\n"
" v_add_f32 v82, v82, v86\n"
" v_mfma_f32_16x16x32_bf16 v[60:63], a[12:15], a[140:143], v[60:63]\n"
" v_add_f32 v83, v83, v87\n"
" s_waitcnt lgkmcnt(0)\n"
" v_add_f32 v88, v88, v92\n"
" s_waitcnt vmcnt(6)\n"
" v_mfma_f32_16x16x32_bf16 v[48:51], a[192:195], a[144:147], v[48:51]\n"
" s_add_u32 s68, s90, 288768\n"
" buffer_load_dwordx4 a[172:175], v1, s[16:19], s68 offen sc1\n"
" v_add_f32 v89, v89, v93\n"
" v_mfma_f32_16x16x32_bf16 v[52:55], a[196:199], a[144:147], v[52:55]\n"
" v_add_f32 v90, v90, v94\n"
" v_add_f32 v91, v91, v95\n"
" v_mfma_f32_16x16x32_bf16 v[56:59], a[200:203], a[144:147], v[56:59]\n"
" s_add_u32 s68, s90, 292864\n"
" buffer_load_dwordx4 a[176:179], v1, s[16:19], s68 offen sc1\n"
" v_add_f32_e64 v80, v80, v88\n"
" v_mfma_f32_16x16x32_bf16 v[60:63], a[204:207], a[144:147], v[60:63]\n"
" s_add_u32 s68, s90, 296960\n"
" buffer_load_dwordx4 a[180:183], v1, s[16:19], s68 offen sc1\n"
" s_add_u32 s68, s90, 301056\n"
" buffer_load_dwordx4 a[184:187], v1, s[16:19], s68 offen sc1\n"
" s_add_u32 s68, s90, 305152\n"
" s_add_u32 s68, s68, s89\n"
" buffer_load_dwordx4 a[188:191], v1, s[16:19], s68 offen sc1\n"
" v_add_f32 v81, v81, v89\n"
" v_add_f32 v82, v82, v90\n"
" v_add_f32 v83, v83, v91\n"
" v_fma_f32 v80, v80, v112, v100\n"
" v_fma_f32 v81, v81, v113, v101\n"
" v_fma_f32 v82, v82, v114, v102\n"
" v_fma_f32 v83, v83, v115, v103\n"
" v_cvt_pk_bf16_f32 v118, v80, v81\n"
" v_cvt_pk_bf16_f32 v119, v82, v83\n"
" s_add_u32 s65, s87, 0\n"
" buffer_store_dwordx2 v[116:117], v11, s[8:11], s65 offen sc1\n"
" s_add_u32 s65, s87, 16384\n"
" buffer_store_dwordx2 v[118:119], v11, s[8:11], s65 offen sc1\n"
" s_waitcnt vmcnt(12)\n"
" v_mfma_f32_16x16x32_bf16 v[64:67], a[208:211], a[148:151], 0\n"
" v_mfma_f32_16x16x32_bf16 v[68:71], a[212:215], a[148:151], 0\n"
" v_mfma_f32_16x16x32_bf16 v[72:75], a[216:219], a[148:151], 0\n"
" v_mfma_f32_16x16x32_bf16 v[76:79], a[220:223], a[148:151], 0\n"
" ds_write_b128 v12, v[48:51] offset:32768\n"
" s_waitcnt vmcnt(11)\n"
" v_mfma_f32_16x16x32_bf16 v[64:67], a[224:227], a[152:155], v[64:67]\n"
" v_mfma_f32_16x16x32_bf16 v[68:71], a[228:231], a[152:155], v[68:71]\n"
" v_mfma_f32_16x16x32_bf16 v[72:75], a[232:235], a[152:155], v[72:75]\n"
" ds_write_b128 v13, v[52:55] offset:33792\n"
" v_mfma_f32_16x16x32_bf16 v[76:79], a[236:239], a[152:155], v[76:79]\n"
" s_waitcnt vmcnt(10)\n"
" v_mfma_f32_16x16x32_bf16 v[64:67], a[240:243], a[156:159], v[64:67]\n"
" v_mfma_f32_16x16x32_bf16 v[68:71], a[244:247], a[156:159], v[68:71]\n"
" ds_write_b128 v14, v[56:59] offset:34816\n"
" v_mfma_f32_16x16x32_bf16 v[72:75], a[248:251], a[156:159], v[72:75]\n"
" v_mfma_f32_16x16x32_bf16 v[76:79], a[252:255], a[156:159], v[76:79]\n"
" s_waitcnt vmcnt(9)\n"
" v_mfma_f32_16x16x32_bf16 v[64:67], v[128:131], a[160:163], v[64:67]\n"
" ds_write_b128 v15, v[60:63] offset:35840\n"
" v_mfma_f32_16x16x32_bf16 v[68:71], v[132:135], a[160:163], v[68:71]\n"
" v_mfma_f32_16x16x32_bf16 v[72:75], v[136:139], a[160:163], v[72:75]\n"
" v_mfma_f32_16x16x32_bf16 v[76:79], v[140:143], a[160:163], v[76:79]\n"
" s_waitcnt lgkmcnt(0)\n"
" s_barrier\n"
" s_waitcnt vmcnt(8)\n"
" v_mfma_f32_16x16x32_bf16 v[64:67], v[144:147], a[164:167], v[64:67]\n"
" ds_read_b128 v[208:211], v9 offset:32768\n"
" v_mfma_f32_16x16x32_bf16 v[68:71], v[148:151], a[164:167], v[68:71]\n"
" ds_read_b128 v[212:215], v9 offset:36864\n"
" v_mfma_f32_16x16x32_bf16 v[72:75], v[152:155], a[164:167], v[72:75]\n"
" ds_read_b128 v[216:219], v9 offset:40960\n"
" v_mfma_f32_16x16x32_bf16 v[76:79], v[156:159], a[164:167], v[76:79]\n"
" ds_read_b128 v[220:223], v9 offset:45056\n"
" s_waitcnt vmcnt(7)\n"
" v_mfma_f32_16x16x32_bf16 v[64:67], v[160:163], a[168:171], v[64:67]\n"
" v_mfma_f32_16x16x32_bf16 v[68:71], v[164:167], a[168:171], v[68:71]\n"
" v_mfma_f32_16x16x32_bf16 v[72:75], v[168:171], a[168:171], v[72:75]\n"
" v_mfma_f32_16x16x32_bf16 v[76:79], v[172:175], a[168:171], v[76:79]\n"
" s_waitcnt vmcnt(6)\n"
" v_mfma_f32_16x16x32_bf16 v[64:67], v[176:179], a[172:175], v[64:67]\n"
" v_mfma_f32_16x16x32_bf16 v[68:71], v[180:183], a[172:175], v[68:71]\n"
" s_waitcnt lgkmcnt(2)\n"
" v_add_f32 v208, v208, v212\n"
" v_add_f32 v209, v209, v213\n"
" v_mfma_f32_16x16x32_bf16 v[72:75], v[184:187], a[172:175], v[72:75]\n"
" v_add_f32 v210, v210, v214\n"
" v_add_f32 v211, v211, v215\n"
" v_mfma_f32_16x16x32_bf16 v[76:79], v[188:191], a[172:175], v[76:79]\n"
" s_waitcnt lgkmcnt(0)\n"
" v_add_f32 v216, v216, v220\n"
" v_add_f32 v217, v217, v221\n"
" s_waitcnt vmcnt(5)\n"
" v_mfma_f32_16x16x32_bf16 v[64:67], v[192:195], a[176:179], v[64:67]\n"
" v_add_f32 v218, v218, v222\n"
" v_add_f32 v219, v219, v223\n"
" v_mfma_f32_16x16x32_bf16 v[68:71], v[196:199], a[176:179], v[68:71]\n"
" v_add_f32 v208, v208, v216\n"
" v_add_f32 v209, v209, v217\n"
" v_mfma_f32_16x16x32_bf16 v[72:75], v[200:203], a[176:179], v[72:75]\n"
" v_add_f32 v210, v210, v218\n"
" v_add_f32_e64 v211, v211, v219\n"
" v_mfma_f32_16x16x32_bf16 v[76:79], v[204:207], a[176:179], v[76:79]\n"
" v_fma_f32 v208, v208, v112, v104\n"
" v_fma_f32 v209, v209, v113, v105\n"
" s_waitcnt vmcnt(4)\n"
" v_mfma_f32_16x16x32_bf16 v[64:67], v[240:243], a[180:183], v[64:67]\n"
" v_fma_f32 v210, v210, v114, v106\n"
" v_fma_f32 v211, v211, v115, v107\n"
" v_mfma_f32_16x16x32_bf16 v[68:71], v[244:247], a[180:183], v[68:71]\n"
" s_add_u32 s65, s87, 32768\n"
" v_cvt_pk_bf16_f32 v120, v208, v209\n"
" v_cvt_pk_bf16_f32 v121, v210, v211\n"
" buffer_store_dwordx2 v[120:121], v11, s[8:11], s65 offen sc1\n"
" v_mfma_f32_16x16x32_bf16 v[72:75], v[248:251], a[180:183], v[72:75]\n"
" v_mfma_f32_16x16x32_bf16 v[76:79], v[124:127], a[180:183], v[76:79]\n"
" s_waitcnt vmcnt(4)\n"
" v_mfma_f32_16x16x32_bf16 v[64:67], a[0:3], a[184:187], v[64:67]\n"
" v_mfma_f32_16x16x32_bf16 v[68:71], a[4:7], a[184:187], v[68:71]\n"
" v_mfma_f32_16x16x32_bf16 v[72:75], a[8:11], a[184:187], v[72:75]\n"
" v_mfma_f32_16x16x32_bf16 v[76:79], a[12:15], a[184:187], v[76:79]\n"
" s_waitcnt vmcnt(3)\n"
" v_mfma_f32_16x16x32_bf16 v[64:67], a[192:195], a[188:191], v[64:67]\n"
" v_mfma_f32_16x16x32_bf16 v[68:71], a[196:199], a[188:191], v[68:71]\n"
" v_mfma_f32_16x16x32_bf16 v[72:75], a[200:203], a[188:191], v[72:75]\n"
" v_mfma_f32_16x16x32_bf16 v[76:79], a[204:207], a[188:191], v[76:79]\n"
" s_nop 4\n"
" ds_write_b128 v12, v[64:67] offset:49152\n"
" ds_write_b128 v13, v[68:71] offset:50176\n"
" ds_write_b128 v14, v[72:75] offset:51200\n"
" ds_write_b128 v15, v[76:79] offset:52224\n"
" s_waitcnt lgkmcnt(0)\n"
" s_barrier\n"
" ds_read_b128 v[80:83], v9 offset:49152\n"
" ds_read_b128 v[84:87], v9 offset:53248\n"
" ds_read_b128 v[88:91], v9 offset:57344\n"
" ds_read_b128 v[92:95], v9 offset:61440\n"
" s_waitcnt lgkmcnt(2)\n"
" v_pk_add_f32 v[80:81], v[80:81], v[84:85]\n"
" v_pk_add_f32 v[82:83], v[82:83], v[86:87]\n"
" s_waitcnt lgkmcnt(0)\n"
" v_pk_add_f32 v[88:89], v[88:89], v[92:93]\n"
" v_pk_add_f32 v[90:91], v[90:91], v[94:95]\n"
" v_pk_add_f32 v[80:81], v[80:81], v[88:89]\n"
" v_pk_add_f32 v[82:83], v[82:83], v[90:91]\n"
" v_pk_fma_f32 v[80:81], v[80:81], v[112:113], v[108:109]\n"
" v_pk_fma_f32 v[82:83], v[82:83], v[114:115], v[110:111]\n"
" v_cvt_pk_bf16_f32 v122, v80, v81\n"
" v_cvt_pk_bf16_f32 v123, v82, v83\n"
" s_add_u32 s65, s87, 49152\n"
" buffer_store_dwordx2 v[122:123], v11, s[8:11], s65 offen sc1\n"
" s_or_b32 s65, s2, s3\n"
" s_cmp_eq_u32 s65, 0\n"
" s_cbranch_scc0 .LL_ep\n"
" v_mov_b32 v4, s48\n"
" v_mov_b32_e64 v5, s49\n"
" s_mov_b32 exec_lo, 1\n"
" s_mov_b32 exec_hi, 0\n"
" buffer_store_dwordx2 v[4:5], off, s[16:19], 0 sc1\n"
" s_mov_b64 exec, -1\n"
".LL_ep:\n"
" s_endpgm\n"
".LmodA_slow:\n"
" s_waitcnt lgkmcnt(0)\n"
" s_nop 7\n"
" s_add_u32 s96, s96, 1\n"
" s_cmp_gt_u32 s96, 60000\n"
" s_cbranch_scc1 .LmodA_ok\n"
" s_mov_b32 s67, 8192\n"
" buffer_load_dwordx4 v[212:215], v181, s[16:19], s67 offen sc1\n"
" s_mov_b32 s67, 20480\n"
" buffer_load_dwordx4 v[216:219], v181, s[16:19], s67 offen sc1\n"
" s_mov_b32 s67, 32768\n"
" buffer_load_dwordx4 v[220:223], v181, s[16:19], s67 offen sc1\n"
" s_mov_b32 s67, 45056\n"
" buffer_load_dwordx4 v[224:227], v181, s[16:19], s67 offen sc1\n"
" s_waitcnt vmcnt(0) lgkmcnt(0)\n"
" s_branch .LmodA_chk\n"
".Lf1_slow:\n"
" s_mov_b32 s67, 0\n"
" s_mov_b32 exec_lo, 0x03ff00ff\n"
" s_mov_b32 exec_hi, 0xff\n"
" buffer_load_dword v234, v238, s[16:19], s67 offen sc1\n"
" s_sleep 3\n"
" buffer_load_dword v235, v238, s[16:19], s67 offen sc1\n"
".Lf1_loop:\n"
" s_waitcnt vmcnt(1)\n"
" v_cmp_ne_u32_e64 vcc, s48, v234\n"
" s_nop 0\n"
" s_cbranch_vccz .Lf1_done\n"
" buffer_load_dword v234, v238, s[16:19], s67 offen sc1\n"
" s_waitcnt vmcnt(1)\n"
" v_cmp_ne_u32 vcc, s48, v235\n"
" s_nop 0\n"
" s_cbranch_vccz .Lf1_done\n"
" buffer_load_dword v235, v238, s[16:19], s67 offen sc1\n"
" s_add_u32 s96, s96, 1\n"
" s_cmp_gt_u32 s96, 60000\n"
" s_cbranch_scc0 .Lf1_loop\n"
".Lf1_done:\n"
" s_mov_b64 exec, -1\n"
" s_nop 1\n"
" s_branch .Lf1_ok\n"
".Lkvp_slow:\n"
" s_waitcnt lgkmcnt(0)\n"
" s_nop 7\n"
" s_add_u32 s96, s96, 1\n"
" s_cmp_gt_u32 s96, 60000\n"
" s_cbranch_scc1 .Lkvp_ok\n"
" buffer_load_dwordx4 v[80:83], v1, s[16:19], s69 offen offset:0 sc1\n"
" buffer_load_dwordx4 v[84:87], v1, s[16:19], s69 offen offset:1024 sc1\n"
" s_waitcnt vmcnt(0) lgkmcnt(0)\n"
" s_branch .Lkvp_chk\n"
".Lkzp_slow:\n"
" s_waitcnt lgkmcnt(0)\n"
" s_nop 7\n"
" s_add_u32 s96, s96, 1\n"
" s_cmp_gt_u32 s96, 60000\n"
" s_cbranch_scc1 .Lkzp_ok\n"
" s_lshl_b32 s67, s59, 12\n"
" s_add_u32 s67, s67, 163840\n"
" buffer_load_dwordx4 v[16:19], v1, s[16:19], s67 offen offset:0 sc1\n"
" buffer_load_dwordx4 v[20:23], v1, s[16:19], s67 offen offset:1024 sc1\n"
" buffer_load_dwordx4 v[24:27], v1, s[16:19], s67 offen offset:2048 sc1\n"
" buffer_load_dwordx4 v[28:31], v1, s[16:19], s67 offen offset:3072 sc1\n"
" s_waitcnt vmcnt(0) lgkmcnt(0)\n"
" s_branch .Lkzp_chk\n"
".Lkvr_slow:\n"
" s_waitcnt lgkmcnt(0)\n"
" s_nop 7\n"
" s_add_u32 s96, s96, 1\n"
" s_cmp_gt_u32 s96, 60000\n"
" s_cbranch_scc1 .Lkvr_ok\n"
" s_lshl_b32 s67, s59, 11\n"
" s_add_u32 s67, s67, 65536\n"
" buffer_load_dwordx4 v[32:35], v194, s[16:19], s67 offen offset:0 sc1\n"
" buffer_load_dwordx4 v[36:39], v194, s[16:19], s67 offen offset:256 sc1\n"
" s_waitcnt vmcnt(0) lgkmcnt(0)\n"
" s_branch .Lkvr_chk\n"
".LmodB_slow:\n"
" s_waitcnt lgkmcnt(0)\n"
" s_nop 7\n"
" s_add_u32 s96, s96, 1\n"
" s_cmp_gt_u32 s96, 60000\n"
" s_cbranch_scc1 .LmodB_ok\n"
" s_mov_b32 s67, 14336\n"
" buffer_load_dwordx4 v[212:215], v181, s[16:19], s67 offen sc1\n"
" s_mov_b32 s67, 26624\n"
" buffer_load_dwordx4 v[216:219], v181, s[16:19], s67 offen sc1\n"
" s_mov_b32 s67, 38912\n"
" buffer_load_dwordx4 v[220:223], v181, s[16:19], s67 offen sc1\n"
" s_mov_b32 s67, 51200\n"
" buffer_load_dwordx4 v[224:227], v181, s[16:19], s67 offen sc1\n"
" s_lshl2_add_u32 s67, s58, s3\n"
" s_lshl_b32 s67, s67, 6\n"
" s_add_u32 s67, s67, 12288\n"
" buffer_load_dwordx4 v[208:211], v193, s[16:19], s67 offen sc1\n"
" s_waitcnt vmcnt(0) lgkmcnt(0)\n"
" s_branch .LmodB_chk\n"
".Lf4_slow:\n"
" s_mov_b32 s67, 16252928\n"
" s_mov_b32 exec_lo, 0\n"
" s_mov_b32 exec_hi, s61\n"
" buffer_load_dword v234, v238, s[16:19], s67 offen sc1\n"
" s_sleep 3\n"
" buffer_load_dword v235, v238, s[16:19], s67 offen sc1\n"
".Lf4_loop:\n"
" s_waitcnt vmcnt(1)\n"
" v_cmp_ne_u32 vcc, s48, v234\n"
" s_nop 0\n"
" s_cbranch_vccz .Lf4_done\n"
" buffer_load_dword v234, v238, s[16:19], s67 offen sc1\n"
" s_waitcnt vmcnt(1)\n"
" v_cmp_ne_u32 vcc, s48, v235\n"
" s_nop 0\n"
" s_cbranch_vccz .Lf4_done\n"
" buffer_load_dword v235, v238, s[16:19], s67 offen sc1\n"
" s_add_u32 s96, s96, 1\n"
" s_cmp_gt_u32 s96, 60000\n"
" s_cbranch_scc0 .Lf4_loop\n"
".Lf4_done:\n"
" s_mov_b64 exec, -1\n"
" s_nop 1\n"
" s_branch .Lf4_ok\n"
".Lf2_slow:\n"
" s_mov_b32 s67, 32768\n"
" s_mov_b32 exec_lo, 0\n"
" s_mov_b32 exec_hi, 0xffff0000\n"
" buffer_load_dword v234, v238, s[16:19], s67 offen sc1\n"
" s_sleep 3\n"
" buffer_load_dword v235, v238, s[16:19], s67 offen sc1\n"
".Lf2_loop:\n"
" s_waitcnt vmcnt(1)\n"
" v_cmp_ne_u32 vcc, s48, v234\n"
" s_nop 0\n"
" s_cbranch_vccz .Lf2_done\n"
" buffer_load_dword v234, v238, s[16:19], s67 offen sc1\n"
" s_waitcnt vmcnt(1)\n"
" v_cmp_ne_u32 vcc, s48, v235\n"
" s_nop 0\n"
" s_cbranch_vccz .Lf2_done\n"
" buffer_load_dword v235, v238, s[16:19], s67 offen sc1\n"
" s_add_u32 s96, s96, 1\n"
" s_cmp_gt_u32 s96, 60000\n"
" s_cbranch_scc0 .Lf2_loop\n"
".Lf2_done:\n"
" s_mov_b64 exec, -1\n"
" s_nop 1\n"
" s_branch .Lf2_ok\n"
".Lf3_slow:\n"
" s_mov_b32 s67, 65536\n"
" s_mov_b32 exec_lo, 0\n"
" s_mov_b32 exec_hi, 0xffff0000\n"
" buffer_load_dword v234, v238, s[16:19], s67 offen sc1\n"
" s_sleep 3\n"
" buffer_load_dword v235, v238, s[16:19], s67 offen sc1\n"
".Lf3_loop:\n"
" s_waitcnt vmcnt(1)\n"
" v_cmp_ne_u32 vcc, s48, v234\n"
" s_nop 0\n"
" s_cbranch_vccz .Lf3_done\n"
" buffer_load_dword v234, v238, s[16:19], s67 offen sc1\n"
" s_waitcnt vmcnt(1)\n"
" v_cmp_ne_u32 vcc, s48, v235\n"
" s_nop 0\n"
" s_cbranch_vccz .Lf3_done\n"
" buffer_load_dword v235, v238, s[16:19], s67 offen sc1\n"
" s_add_u32 s96, s96, 1\n"
" s_cmp_gt_u32 s96, 60000\n"
" s_cbranch_scc0 .Lf3_loop\n"
".Lf3_done:\n"
" s_mov_b64 exec, -1\n"
" s_nop 1\n"
" s_branch .Lf3_ok\n"
".Lf5_slow:\n"
" s_mov_b32 s67, 16285696\n"
" s_mov_b32 exec_lo, 0\n"
" s_mov_b32 exec_hi, 0xffff0000\n"
" buffer_load_dword v234, v238, s[16:19], s67 offen sc1\n"
" s_sleep 3\n"
" buffer_load_dword v235, v238, s[16:19], s67 offen sc1\n"
".Lf5_loop:\n"
" s_waitcnt vmcnt(1)\n"
" v_cmp_ne_u32 vcc, s48, v234\n"
" s_nop 0\n"
" s_cbranch_vccz .Lf5_done\n"
" buffer_load_dword v234, v238, s[16:19], s67 offen sc1\n"
" s_waitcnt vmcnt(1)\n"
" v_cmp_ne_u32 vcc, s48, v235\n"
" s_nop 0\n"
" s_cbranch_vccz .Lf5_done\n"
" buffer_load_dword v235, v238, s[16:19], s67 offen sc1\n"
" s_add_u32 s96, s96, 1\n"
" s_cmp_gt_u32 s96, 60000\n"
" s_cbranch_scc0 .Lf5_loop\n"
".Lf5_done:\n"
" s_mov_b64 exec, -1\n"
" s_nop 1\n"
" s_branch .Lf5_ok\n"
".Lf6_slow:\n"
" s_mov_b32 s67, 16318464\n"
" s_mov_b32 exec_lo, 0\n"
" s_mov_b32 exec_hi, 0xffff0000\n"
" buffer_load_dword v234, v238, s[16:19], s67 offen sc1\n"
" s_sleep 3\n"
" buffer_load_dword v235, v238, s[16:19], s67 offen sc1\n"
".Lf6_loop:\n"
" s_waitcnt vmcnt(1)\n"
" v_cmp_ne_u32 vcc, s48, v234\n"
" s_nop 0\n"
" s_cbranch_vccz .Lf6_done\n"
" buffer_load_dword v234, v238, s[16:19], s67 offen sc1\n"
" s_waitcnt vmcnt(1)\n"
" v_cmp_ne_u32 vcc, s48, v235\n"
" s_nop 0\n"
" s_cbranch_vccz .Lf6_done\n"
" buffer_load_dword v235, v238, s[16:19], s67 offen sc1\n"
" s_add_u32 s96, s96, 1\n"
" s_cmp_gt_u32 s96, 60000\n"
" s_cbranch_scc0 .Lf6_loop\n"
".Lf6_done:\n"
" s_mov_b64 exec, -1\n"
" s_nop 1\n"
" s_branch .Lf6_ok\n"
".Lk_end:\n"
  :: "{s[0:1]}"(__builtin_amdgcn_kernarg_segment_ptr()), "{s2}"(blockIdx.x), "{v0}"(threadIdx.x), "{s100}"((unsigned)(size_t)lds)
  : "memory", "vcc", "scc", "v1", "v2", "v3", "v4", "v5", "v6", "v7", "v8", "v9", "v10", "v11", "v12", "v13", "v14", "v15", "v16", "v17", "v18", "v19", "v20", "v21", "v22", "v23", "v24", "v25", "v26", "v27", "v28", "v29", "v30", "v31", "v32", "v33", "v34", "v35", "v36", "v37", "v38", "v39", "v40", "v41", "v42", "v43", "v44", "v45", "v46", "v47", "v48", "v49", "v50", "v51", "v52", "v53", "v54", "v55", "v56", "v57", "v58", "v59", "v60", "v61", "v62", "v63", "v64", "v65", "v66", "v67", "v68", "v69", "v70", "v71", "v72", "v73", "v74", "v75", "v76", "v77", "v78", "v79", "v80", "v81", "v82", "v83", "v84", "v85", "v86", "v87", "v88", "v89", "v90", "v91", "v92", "v93", "v94", "v95", "v96", "v97", "v98", "v99", "v100", "v101", "v102", "v103", "v104", "v105", "v106", "v107", "v108", "v109", "v110", "v111", "v112", "v113", "v114", "v115", "v116", "v117", "v118", "v119", "v120", "v121", "v122", "v123", "v124", "v125", "v126", "v127", "v128", "v129", "v130", "v131", "v132", "v133", "v134", "v135", "v136", "v137", "v138", "v139", "v140", "v141", "v142", "v143", "v144", "v145", "v146", "v147", "v148", "v149", "v150", "v151", "v152", "v153", "v154", "v155", "v156", "v157", "v158", "v159", "v160", "v161", "v162", "v163", "v164", "v165", "v166", "v167", "v168", "v169", "v170", "v171", "v172", "v173", "v174", "v175", "v176", "v177", "v178", "v179", "v180", "v181", "v182", "v183", "v184", "v185", "v186", "v187", "v188", "v189", "v190", "v191", "v192", "v193", "v194", "v195", "v196", "v197", "v198", "v199", "v200", "v201", "v202", "v203", "v204", "v205", "v206", "v207", "v208", "v209", "v210", "v211", "v212", "v213", "v214", "v215", "v216", "v217", "v218", "v219", "v220", "v221", "v222", "v223", "v224", "v225", "v226", "v227", "v228", "v229", "v230", "v231", "v232", "v233", "v234", "v235", "v236", "v237", "v238", "v239", "v240", "v241", "v242", "v243", "v244", "v245", "v246", "v247", "v248", "v249", "v250", "v251", "v252", "v253", "v254", "v255", "a0", "a1", "a2", "a3", "a4", "a5", "a6", "a7", "a8", "a9", "a10", "a11", "a12", "a13", "a14", "a15", "a16", "a17", "a18", "a19", "a20", "a21", "a22", "a23", "a24", "a25", "a26", "a27", "a28", "a29", "a30", "a31", "a32", "a33", "a34", "a35", "a36", "a37", "a38", "a39", "a40", "a41", "a42", "a43", "a44", "a45", "a46", "a47", "a48", "a49", "a50", "a51", "a52", "a53", "a54", "a55", "a56", "a57", "a58", "a59", "a60", "a61", "a62", "a63", "a64", "a65", "a66", "a67", "a68", "a69", "a70", "a71", "a72", "a73", "a74", "a75", "a76", "a77", "a78", "a79", "a80", "a81", "a82", "a83", "a84", "a85", "a86", "a87", "a88", "a89", "a90", "a91", "a92", "a93", "a94", "a95", "a96", "a97", "a98", "a99", "a100", "a101", "a102", "a103", "a104", "a105", "a106", "a107", "a108", "a109", "a110", "a111", "a112", "a113", "a114", "a115", "a116", "a117", "a118", "a119", "a120", "a121", "a122", "a123", "a124", "a125", "a126", "a127", "a128", "a129", "a130", "a131", "a132", "a133", "a134", "a135", "a136", "a137", "a138", "a139", "a140", "a141", "a142", "a143", "a144", "a145", "a146", "a147", "a148", "a149", "a150", "a151", "a152", "a153", "a154", "a155", "a156", "a157", "a158", "a159", "a160", "a161", "a162", "a163", "a164", "a165", "a166", "a167", "a168", "a169", "a170", "a171", "a172", "a173", "a174", "a175", "a176", "a177", "a178", "a179", "a180", "a181", "a182", "a183", "a184", "a185", "a186", "a187", "a188", "a189", "a190", "a191", "a192", "a193", "a194", "a195", "a196", "a197", "a198", "a199", "a200", "a201", "a202", "a203", "a204", "a205", "a206", "a207", "a208", "a209", "a210", "a211", "a212", "a213", "a214", "a215", "a216", "a217", "a218", "a219", "a220", "a221", "a222", "a223", "a224", "a225", "a226", "a227", "a228", "a229", "a230", "a231", "a232", "a233", "a234", "a235", "a236", "a237", "a238", "a239", "a240", "a241", "a242", "a243", "a244", "a245", "a246", "a247", "a248", "a249", "a250", "a251", "a252", "a253", "a254", "a255", "s3", "s4", "s5", "s6", "s7", "s8", "s9", "s10", "s11", "s12", "s13", "s14", "s15", "s16", "s17", "s18", "s19", "s20", "s21", "s22", "s23", "s24", "s25", "s26", "s27", "s28", "s29", "s30", "s31", "s33", "s34", "s35", "s36", "s37", "s38", "s39", "s40", "s41", "s42", "s43", "s44", "s45", "s46", "s47", "s48", "s49", "s50", "s51", "s52", "s53", "s54", "s55", "s56", "s57", "s58", "s59", "s60", "s61", "s62", "s63", "s64", "s65", "s66", "s67", "s68", "s69", "s70", "s71", "s72", "s73", "s74", "s75", "s76", "s77", "s78", "s79", "s80", "s81", "s82", "s83", "s84", "s85", "s86", "s87", "s88", "s89", "s90", "s91", "s92", "s93", "s94", "s95", "s96", "s97", "s98", "s99");
#endif
}

extern "C" void kernel_launch(void* const* d_in, const int* in_sizes, int n_in, void* d_out, int out_size, void* d_ws, size_t ws_size,
                              hipStream_t stream) {
  Args a;
  const int order[11] = {9, 8, 0, -2, 1, 5, 6, 7, 2, 3, -1};
  for (int i = 0; i < 11; ++i) {
    const int k = order[i];
    a.t[i].p = k >= 0 ? d_in[k] : (k == -1 ? d_out : (void*)((char*)d_ws + ((size_t)0 << 20)));
    a.t[i].n = k >= 0 ? (unsigned)in_sizes[k] * 2u : (k == -1 ? (unsigned)out_size * 2u : 16777216u);
    a.t[i].f = 0x00020000u;
  }
  fused_block<<<256, 256, 0, stream>>>(a);
}
```
